```python
import torch

def _softmax(x, axis=-1):
    return torch.softmax(x, dim=axis)
def _gelu(x):
    return torch.nn.functional.gelu(x, approximate="tanh")
def _silu(x):
    return torch.nn.functional.silu(x)
def _sigmoid(x):
    return torch.sigmoid(x)
def _layernorm(x, axis=-1, eps=1e-5):
    m = x.mean(dim=axis, keepdim=True)
    v = x.var(dim=axis, keepdim=True, unbiased=False)
    return (x - m) / torch.sqrt(v + eps)
def _scatter_add(tgt, idx, src, axis=0):
    return tgt.clone().index_add_(axis, idx, src)
def _segment_sum(src, seg, n):
    out = torch.zeros((n,) + src.shape[1:], dtype=src.dtype)
    return out.index_add_(0, seg, src)
def _pad(x, pw):
    flat = tuple(v for pair in reversed(pw) for v in pair)
    return torch.nn.functional.pad(x, flat)

kernel_name = 'sequential_preLN__gqa_block_cached__glu_gated_geglu__decode'

def setup_inputs():

    _w = (lambda a: (lambda t: t.to(torch.bfloat16) if t.is_floating_point() else t)(torch.as_tensor(a)))
    def _A_setup():
        B, Sq, D = 1, 1, 1024
        Hq, Hkv, Sk, Dh = 32, 4, 65536, 128
        assert Hq % Hkv == 0, (Hq, Hkv)
        return {
            "x": _w((lambda *s: torch.randn(*s, dtype=torch.bfloat16) * 0.05)(B, Sq, D)),
            "k_cache": _w((lambda *s: torch.randn(*s, dtype=torch.bfloat16) * 3.2)(B, Hkv, Sk, Dh)),
            "v_cache": _w((lambda *s: torch.randn(*s, dtype=torch.bfloat16) * 1.6)(B, Hkv, Sk, Dh)),
            "wq": _w((lambda *s: torch.randn(*s, dtype=torch.bfloat16) * 3.2)(D, Hq * Dh) * (D ** -0.5)),
            "wo": _w((lambda *s: torch.randn(*s, dtype=torch.bfloat16) * 1.6)(Hq * Dh, D) * ((Hq * Dh) ** -0.5)),
        }
    _A_in = _A_setup()

    def _B_setup():
        B, S, Dm, Df = 1, 1, 1024, 3584
        return {
            "x": _w((lambda *s: torch.randn(*s, dtype=torch.bfloat16) * 0.05)(B, S, Dm)),
            "w_gate": _w((lambda *s: torch.randn(*s, dtype=torch.bfloat16) * 1.6)(Dm, Df) * (Dm ** -0.5)),
            "w_up": _w((lambda *s: torch.randn(*s, dtype=torch.bfloat16) * 1.6)(Dm, Df) * (Dm ** -0.5)),
            "w_down": _w((lambda *s: torch.randn(*s, dtype=torch.bfloat16) * 1.6)(Df, Dm) * (Df ** -0.5)),
        }
    _B_in = _B_setup()

    return {
        "A_x": _A_in["x"],
        "A_k_cache": _A_in["k_cache"],
        "A_v_cache": _A_in["v_cache"],
        "A_wq": _A_in["wq"],
        "A_wo": _A_in["wo"],
        "B_x": _B_in["x"],
        "B_w_gate": _B_in["w_gate"],
        "B_w_up": _B_in["w_up"],
        "B_w_down": _B_in["w_down"],
    }

def reference(A_x, A_k_cache, A_v_cache, A_wq, A_wo, B_x, B_w_gate, B_w_up, B_w_down):
    def _A_ref(x, k_cache, v_cache, wq, wo):
        B, Sq, D = x.shape
        Hq, Dh = 32, 128
        Hkv = k_cache.shape[1]
        rep = Hq // Hkv
        q = (lambda x, *d: x.permute(*d))((x @ wq).reshape(B, Sq, Hq, Dh), 0, 2, 1, 3)
        k = (lambda x, n, axis: torch.repeat_interleave(x, n, dim=axis))(k_cache, rep, axis=1)
        v = (lambda x, n, axis: torch.repeat_interleave(x, n, dim=axis))(v_cache, rep, axis=1)
        scores = torch.einsum("bhqd,bhkd->bhqk", q, k) * (Dh ** -0.5)
        p = _softmax(scores, axis=-1)
        o = torch.einsum("bhqk,bhkd->bhqd", p, v)
        return (lambda x, *d: x.permute(*d))(o, 0, 2, 1, 3).reshape(B, Sq, Hq * Dh) @ wo

    def _B_ref(x, w_gate, w_up, w_down):
        g = torch.einsum("bsd,df->bsf", x, w_gate)
        u = torch.einsum("bsd,df->bsf", x, w_up)
        h = _gelu(g) * u
        return torch.einsum("bsf,fd->bsd", h, w_down)

    x = A_x
    h = x + _A_ref(_layernorm(x, axis=-1), A_k_cache, A_v_cache, A_wq, A_wo)
    h2 = h + _B_ref(_layernorm(h, axis=-1), B_w_gate, B_w_up, B_w_down)
    return h2
```

```cpp
#include <hip/hip_runtime.h>
#include <hip/hip_bf16.h>
#include <stdint.h>

typedef unsigned v4u __attribute__((ext_vector_type(4)));
typedef float v4f __attribute__((ext_vector_type(4)));
typedef __bf16 v8bf __attribute__((ext_vector_type(8)));
#define GAS __attribute__((address_space(1)))

#define MIMIC 1

constexpr int D = 1024, HQ = 32, HKV = 4, SK = 65536, DH = 128, DF = 3584;

__device__ __forceinline__ float bf2f(unsigned short b) { return __uint_as_float(((unsigned)b) << 16); }
__device__ __forceinline__ float bflo(unsigned u) { return __uint_as_float(u << 16); }
__device__ __forceinline__ float bfhi(unsigned u) { return __uint_as_float(u & 0xffff0000u); }
__device__ __forceinline__ unsigned short f2bf(float f) { __bf16 b = (__bf16)f; return __builtin_bit_cast(unsigned short, b); }
__device__ __forceinline__ float rbf(float f) { return bf2f(f2bf(f)); }
__device__ __forceinline__ float rb(float f) {
    return rbf(f);
}
__device__ __forceinline__ unsigned pack2(float a, float b) { return (unsigned)f2bf(a) | ((unsigned)f2bf(b) << 16); }

__device__ __forceinline__ float wave_sum(float v) {
#pragma unroll
    for (int m = 32; m >= 1; m >>= 1) v += __shfl_xor(v, m);
    return v;
}

__device__ __forceinline__ void ln_inplace(float* xs, float* red) {
    const int t = threadIdx.x, lane = t & 63, wv = t >> 6;
    float v[4];
#pragma unroll
    for (int i = 0; i < 4; ++i) v[i] = xs[t + 256 * i];
    float s = wave_sum(v[0] + v[1] + v[2] + v[3]);
    if (lane == 0) red[wv] = s;
    __syncthreads();
    const float mean = (red[0] + red[1] + red[2] + red[3]) * (1.0f / D);
    float q = 0.f;
#pragma unroll
    for (int i = 0; i < 4; ++i) { const float d = v[i] - mean; q += d * d; }
    q = wave_sum(q);
    if (lane == 0) red[4 + wv] = q;
    __syncthreads();
    const float var = (red[4] + red[5] + red[6] + red[7]) * (1.0f / D);
    const float mb = rb(mean);
    const float sd = rb(sqrtf(rb(rb(var) + 1e-5f)));
#pragma unroll
    for (int i = 0; i < 4; ++i) xs[t + 256 * i] = rbf(rb(v[i] - mb) / sd);
    __syncthreads();
}

template <int NL>
__device__ __forceinline__ void gemv64(const unsigned short* W, int ldw, const float* xs, float* red  , float* outp) {
    const int t = threadIdx.x, lane = t & 63, wv = t >> 6;
    const int c8 = lane & 7, r0 = wv * 8 + (lane >> 3);
    v4u w[NL];
#pragma unroll
    for (int i = 0; i < NL; ++i) w[i] = __builtin_nontemporal_load((const GAS v4u*)(W + (size_t)(r0 + 32 * i) * ldw + c8 * 8));
    float a[8] = {0, 0, 0, 0, 0, 0, 0, 0};
#pragma unroll
    for (int i = 0; i < NL; ++i) {
        const float x = xs[r0 + 32 * i];
#pragma unroll
        for (int j = 0; j < 4; ++j) { a[2 * j] += x * bflo(w[i][j]); a[2 * j + 1] += x * bfhi(w[i][j]); }
    }
#pragma unroll
    for (int j = 0; j < 8; ++j) { a[j] += __shfl_xor(a[j], 8); a[j] += __shfl_xor(a[j], 16); a[j] += __shfl_xor(a[j], 32); }
    if (lane < 8) {
#pragma unroll
        for (int j = 0; j < 8; ++j) red[wv * 64 + c8 * 8 + j] = a[j];
    }
    __syncthreads();
    if (t < 64) outp[t] = red[t] + red[64 + t] + red[128 + t] + red[192 + t];
}

__global__ void __launch_bounds__(256) k_qproj(const unsigned short* __restrict__ x, const unsigned short* __restrict__ wq, float* __restrict__ qpart) {
    __shared__ float xs[D];
    __shared__ float red[256];
    const int t = threadIdx.x;
#pragma unroll
    for (int i = 0; i < 4; ++i) xs[t + 256 * i] = bf2f(x[t + 256 * i]);
    __syncthreads();
    ln_inplace(xs, red);
    const int cb = blockIdx.x & 63, rbk = blockIdx.x >> 6;
    gemv64<8>(wq + (size_t)rbk * 256 * 4096 + cb * 64, 4096, xs + rbk * 256, red, qpart + rbk * 4096 + cb * 64);
}

constexpr int NSPLIT = 64;
constexpr int AW = 8;
__global__ void __launch_bounds__(512) k_attn(const unsigned short* __restrict__ K, const unsigned short* __restrict__ V, const float* __restrict__ qpart,
                                              float* __restrict__ pm, float* __restrict__ pl, float* __restrict__ pacc) {
    __shared__ float sacc[AW][8][128];
    __shared__ float sm[AW][8], sl[AW][8];
    const int t = threadIdx.x, lane = t & 63, wv = __builtin_amdgcn_readfirstlane(t >> 6);
    const int g = blockIdx.x >> 6, sp = blockIdx.x & 63;
    const int c = lane & 15, lg = lane >> 4;
    const size_t key0 = (size_t)g * SK + sp * 1024 + wv * 128;
    const GAS v4u* Kp = (const GAS v4u*)(K + key0 * DH);
    const GAS v4u* Vp = (const GAS v4u*)(V + key0 * DH);
    const int koff = (4 * (c & 3) + (c >> 2)) * 16 + lg;
    const int voff = lane;
    v4u kb[2][8], vb[2][8];
#pragma unroll
    for (int i = 0; i < 8; ++i) kb[0][i] = __builtin_nontemporal_load(Kp + koff + (i >> 2) * 256 + (i & 3) * 4);
#pragma unroll
    for (int j = 0; j < 8; ++j) vb[0][j] = __builtin_nontemporal_load(Vp + voff + j * 64);
    v4u qf[4];
#pragma unroll
    for (int i = 0; i < 4; ++i) {
        float qv[8];
        const int idx = (g * 8 + (c & 7)) * 128 + 8 * (4 * i + lg);
#pragma unroll
        for (int j = 0; j < 8; ++j) qv[j] = qpart[idx + j] + qpart[4096 + idx + j] + qpart[8192 + idx + j] + qpart[12288 + idx + j];
#pragma unroll
        for (int j = 0; j < 4; ++j) qf[i][j] = pack2(qv[2 * j], qv[2 * j + 1]);
    }
    float m = -INFINITY, l = 0.f;
    v4f acc[8];
#pragma unroll
    for (int i = 0; i < 8; ++i) acc[i] = (v4f){0, 0, 0, 0};
    const float scale = 0.08838834764831845f;
    const float LOG2E = 1.4426950408889634f;
#pragma unroll
    for (int st = 0; st < 4; ++st) {
        const int cur = st & 1, nxt = cur ^ 1;
        if (st + 1 < 4) {
#pragma unroll
            for (int i = 0; i < 8; ++i) kb[nxt][i] = __builtin_nontemporal_load(Kp + (st + 1) * 512 + koff + (i >> 2) * 256 + (i & 3) * 4);
#pragma unroll
            for (int j = 0; j < 8; ++j) vb[nxt][j] = __builtin_nontemporal_load(Vp + (st + 1) * 512 + voff + j * 64);
        }
        __builtin_amdgcn_sched_barrier(0);
        v4f s0 = {0, 0, 0, 0}, s1 = {0, 0, 0, 0};
#pragma unroll
        for (int i = 0; i < 4; ++i) {
            s0 = __builtin_amdgcn_mfma_f32_16x16x32_bf16(__builtin_bit_cast(v8bf, kb[cur][i]), __builtin_bit_cast(v8bf, qf[i]), s0, 0, 0, 0);
            s1 = __builtin_amdgcn_mfma_f32_16x16x32_bf16(__builtin_bit_cast(v8bf, kb[cur][4 + i]), __builtin_bit_cast(v8bf, qf[i]), s1, 0, 0, 0);
        }
        float s[8];
#pragma unroll
        for (int j = 0; j < 4; ++j) { s[j] = rb(rb(s0[j]) * scale); s[4 + j] = rb(rb(s1[j]) * scale); }
        float mx = fmaxf(fmaxf(fmaxf(s[0], s[1]), fmaxf(s[2], s[3])), fmaxf(fmaxf(s[4], s[5]), fmaxf(s[6], s[7])));
        mx = fmaxf(mx, __shfl_xor(mx, 16));
        mx = fmaxf(mx, __shfl_xor(mx, 32));
        const float mn = fmaxf(m, mx);
        const float alpha = __builtin_amdgcn_exp2f((m - mn) * LOG2E);
        m = mn;
        float p[8], ps = 0.f;
#pragma unroll
        for (int j = 0; j < 8; ++j) { p[j] = __builtin_amdgcn_exp2f((s[j] - mn) * LOG2E); ps += p[j]; }
        l = l * alpha + ps;
        v4u pf;
#pragma unroll
        for (int j = 0; j < 4; ++j) pf[j] = pack2(p[2 * j], p[2 * j + 1]);
#pragma unroll
        for (int i = 0; i < 8; ++i) {
            v4u af;
#pragma unroll
            for (int r = 0; r < 4; ++r)
                af[r] = __builtin_amdgcn_perm(vb[cur][2 * r + 1][i >> 1], vb[cur][2 * r][i >> 1], (i & 1) ? 0x07060302u : 0x05040100u);
            acc[i] *= alpha;
            acc[i] = __builtin_amdgcn_mfma_f32_16x16x32_bf16(__builtin_bit_cast(v8bf, af), __builtin_bit_cast(v8bf, pf), acc[i], 0, 0, 0);
        }
    }
    l += __shfl_xor(l, 16);
    l += __shfl_xor(l, 32);
    if (c < 8) {
        if (lg == 0) { sm[wv][c] = m; sl[wv][c] = l; }
#pragma unroll
        for (int i = 0; i < 8; ++i)
#pragma unroll
            for (int r = 0; r < 4; ++r) sacc[wv][c][8 * (4 * lg + r) + i] = acc[i][r];
    }
    __syncthreads();
    {
        const int h = wv;
        float M = sm[0][h];
#pragma unroll
        for (int w = 1; w < AW; ++w) M = fmaxf(M, sm[w][h]);
        float L = 0.f, a0 = 0.f, a1 = 0.f;
#pragma unroll
        for (int w = 0; w < AW; ++w) {
            const float f = __builtin_amdgcn_exp2f((sm[w][h] - M) * LOG2E);
            L += f * sl[w][h]; a0 += f * sacc[w][h][2 * lane]; a1 += f * sacc[w][h][2 * lane + 1];
        }
        const int hh = g * 8 + h;
        if (lane == 0) { pm[hh * NSPLIT + sp] = M; pl[hh * NSPLIT + sp] = L; }
        *(float2*)&pacc[((size_t)hh * NSPLIT + sp) * 128 + 2 * lane] = make_float2(a0, a1);
    }
}

__global__ void __launch_bounds__(128) k_comb(const float* __restrict__ pm, const float* __restrict__ pl, const float* __restrict__ pacc, float* __restrict__ o) {
    const int h = blockIdx.x, t = threadIdx.x;
    const float LOG2E = 1.4426950408889634f;
    float M = -INFINITY;
    for (int s = 0; s < NSPLIT; ++s) M = fmaxf(M, pm[h * NSPLIT + s]);
    float L = 0.f, a = 0.f;
#pragma unroll 8
    for (int s = 0; s < NSPLIT; ++s) {
        const float f = __builtin_amdgcn_exp2f((pm[h * NSPLIT + s] - M) * LOG2E);
        L += f * pl[h * NSPLIT + s];
        a += f * pacc[((size_t)h * NSPLIT + s) * 128 + t];
    }
    o[h * 128 + t] = rbf(a / L);
}

__global__ void __launch_bounds__(256) k_wo(const float* __restrict__ o, const unsigned short* __restrict__ wo, float* __restrict__ part) {
    __shared__ float xs[256];
    __shared__ float red[256];
    const int t = threadIdx.x;
    const int cb = blockIdx.x & 15, rbk = blockIdx.x >> 4;
    xs[t] = o[rbk * 256 + t];
    __syncthreads();
    gemv64<8>(wo + (size_t)rbk * 256 * 1024 + cb * 64, 1024, xs, red, part + rbk * 1024 + cb * 64);
}

__device__ __forceinline__ float gelu_tanh(float x) {
    const float k0 = 0.7978845608028654f, k1 = 0.044715f;
    const float inner = k0 * (x + k1 * x * x * x);
    return 0.5f * x * (1.0f + tanhf(inner));
}

__global__ void __launch_bounds__(256) k_ffn(const unsigned short* __restrict__ x, const float* __restrict__ part, const unsigned short* __restrict__ wg,
                                             const unsigned short* __restrict__ wu, const unsigned short* __restrict__ wd, float* __restrict__ hbuf, float* __restrict__ dpart) {
    __shared__ float xs[D];
    __shared__ float red[512];
    __shared__ float act[16];
    const int t = threadIdx.x, lane = t & 63, wv = t >> 6, b = blockIdx.x;
    v4u g[8], u[8];
#pragma unroll
    for (int i = 0; i < 8; ++i) {
        const size_t off = (size_t)((t >> 1) + 128 * i) * DF + b * 16 + (t & 1) * 8;
        g[i] = __builtin_nontemporal_load((const GAS v4u*)(wg + off));
        u[i] = __builtin_nontemporal_load((const GAS v4u*)(wu + off));
    }
    v4u dn[8];
#pragma unroll
    for (int i = 0; i < 8; ++i) dn[i] = __builtin_nontemporal_load((const GAS v4u*)(wd + (size_t)(b * 16 + (t >> 7) + 2 * i) * D + (t & 127) * 8));
#pragma unroll
    for (int i = 0; i < 4; ++i) {
        const int j = t + 256 * i;
        float a = 0.f;
#pragma unroll
        for (int r = 0; r < 16; ++r) a += part[r * 1024 + j];
        const float h = rbf(bf2f(x[j]) + rb(a));
        xs[j] = h;
        if (b == 0) hbuf[j] = h;
    }
    __syncthreads();
    ln_inplace(xs, red);
    float ag[8] = {0, 0, 0, 0, 0, 0, 0, 0}, au[8] = {0, 0, 0, 0, 0, 0, 0, 0};
#pragma unroll
    for (int i = 0; i < 8; ++i) {
        const float xv = xs[(t >> 1) + 128 * i];
#pragma unroll
        for (int j = 0; j < 4; ++j) {
            ag[2 * j] += xv * bflo(g[i][j]); ag[2 * j + 1] += xv * bfhi(g[i][j]);
            au[2 * j] += xv * bflo(u[i][j]); au[2 * j + 1] += xv * bfhi(u[i][j]);
        }
    }
#pragma unroll
    for (int j = 0; j < 8; ++j) {
#pragma unroll
        for (int mm = 2; mm <= 32; mm <<= 1) { ag[j] += __shfl_xor(ag[j], mm); au[j] += __shfl_xor(au[j], mm); }
    }
    if (lane < 2) {
#pragma unroll
        for (int j = 0; j < 8; ++j) { red[wv * 32 + lane * 8 + j] = ag[j]; red[wv * 32 + 16 + lane * 8 + j] = au[j]; }
    }
    __syncthreads();
    if (t < 16) {
        const float gg = rb(red[t] + red[32 + t] + red[64 + t] + red[96 + t]);
        const float uu = rb(red[16 + t] + red[48 + t] + red[80 + t] + red[112 + t]);
        act[t] = rbf(rb(gelu_tanh(gg)) * uu);
    }
    __syncthreads();
    float a[8] = {0, 0, 0, 0, 0, 0, 0, 0};
#pragma unroll
    for (int i = 0; i < 8; ++i) {
        const float xv = act[(t >> 7) + 2 * i];
#pragma unroll
        for (int j = 0; j < 4; ++j) { a[2 * j] += xv * bflo(dn[i][j]); a[2 * j + 1] += xv * bfhi(dn[i][j]); }
    }
    float* dp = dpart + ((size_t)b * 2 + (t >> 7)) * 1024 + (t & 127) * 8;
    *(v4f*)dp = (v4f){a[0], a[1], a[2], a[3]};
    *(v4f*)(dp + 4) = (v4f){a[4], a[5], a[6], a[7]};
}

__global__ void __launch_bounds__(256) k_fin(const float* __restrict__ hbuf, const float* __restrict__ dpart, unsigned short* __restrict__ out) {
    __shared__ float red[256];
    const int t = threadIdx.x, c = t & 15, r0 = t >> 4, b = blockIdx.x;
    float a = 0.f;
#pragma unroll
    for (int i = 0; i < 28; ++i) a += dpart[(size_t)(r0 + 16 * i) * 1024 + b * 16 + c];
    red[t] = a;
    __syncthreads();
    if (t < 16) {
        float s = 0.f;
#pragma unroll
        for (int i = 0; i < 16; ++i) s += red[i * 16 + t];
        out[b * 16 + t] = f2bf(hbuf[b * 16 + t] + rb(s));
    }
}

extern "C" void kernel_launch(void* const* d_in, const int* in_sizes, int n_in, void* d_out, int out_size, void* d_ws, size_t ws_size, hipStream_t stream) {
    const unsigned short* x = (const unsigned short*)d_in[0];
    const unsigned short* K = (const unsigned short*)d_in[1];
    const unsigned short* V = (const unsigned short*)d_in[2];
    const unsigned short* wq = (const unsigned short*)d_in[3];
    const unsigned short* wo = (const unsigned short*)d_in[4];
    const unsigned short* wg = (const unsigned short*)d_in[6];
    const unsigned short* wu = (const unsigned short*)d_in[7];
    const unsigned short* wd = (const unsigned short*)d_in[8];
    float* ws = (float*)d_ws;
    float* qpart = ws;
    float* pm = qpart + 4 * 4096;
    float* pl = pm + 2048;
    float* pacc = pl + 2048;
    float* o = pacc + 32 * 64 * 128;
    float* part = o + 4096;
    float* hbuf = part + 16 * 1024;
    float* dpart = hbuf + 1024;
    k_qproj<<<256, 256, 0, stream>>>(x, wq, qpart);
    k_attn<<<256, 512, 0, stream>>>(K, V, qpart, pm, pl, pacc);
    k_comb<<<32, 128, 0, stream>>>(pm, pl, pacc, o);
    k_wo<<<256, 256, 0, stream>>>(o, wo, part);
    k_ffn<<<224, 256, 0, stream>>>(x, part, wg, wu, wd, hbuf, dpart);
    k_fin<<<64, 256, 0, stream>>>(hbuf, dpart, (unsigned short*)d_out);
}
```

```cpp
#include <hip/hip_runtime.h>
#include <stdint.h>

typedef unsigned v4u __attribute__((ext_vector_type(4)));
typedef unsigned v2u __attribute__((ext_vector_type(2)));
typedef float v4f __attribute__((ext_vector_type(4)));
typedef __bf16 v8bf __attribute__((ext_vector_type(8)));
typedef short v4s __attribute__((ext_vector_type(4)));
typedef unsigned long long u64;
#define GAS __attribute__((address_space(1)))
#define LAS __attribute__((address_space(3)))
#define RLX_AGENT __ATOMIC_RELAXED, __HIP_MEMORY_SCOPE_AGENT

#define W_START 20
#define W_MAIN 32
#define W_TAIL 16
#define N_START 4

constexpr int NWG = 256, NFFN = 224, NT = 32;
constexpr int NSLOT = 9, SLOT = 16384;
constexpr unsigned WS_EPOCH = 0, WS_FLAG = 4096, WS_QP = WS_FLAG + 256 * 128, WS_ACC = WS_QP + 4 * 4096 * 4, WS_ML = WS_ACC + 32 * 64 * 512,
                   WS_P = WS_ML + 256 * 64, WS_X = WS_P + 32 * 4096, WS_Y = WS_X + 56 * 4 * 1280, WS_END = WS_Y + 56 * 4096, WS_STAMP = 4u << 20;
constexpr int L_QI = NSLOT * SLOT, L_XS = L_QI + 8 * 272, L_OS = L_XS + 512, L_HS = L_OS + 256, L_ONE = L_HS + 512, L_WS = L_ONE + 512, L_AS = L_WS + 1024,
              L_CTL = L_AS + 512, L_STAMP = L_CTL + 128, L_END = L_STAMP + 512;
enum { C_FULL = 0, C_DONE0, C_DONE1, C_WQ, C_WO, C_XS, C_QPUB, C_QRDY, C_ALLF, C_APUB, C_OS, C_HS };
static_assert(L_END <= 160 * 1024 && (L_XS & 15) == 0, "lds");
constexpr unsigned SENT = 0xFFFFFFFFu;
constexpr unsigned SPIN = 1u << 22;

__device__ __forceinline__ float bflo(unsigned u) { return __uint_as_float(u << 16); }
__device__ __forceinline__ float bfhi(unsigned u) { return __uint_as_float(u & 0xffff0000u); }
__device__ __forceinline__ unsigned short f2bf(float f) { __bf16 b = (__bf16)f; return __builtin_bit_cast(unsigned short, b); }
__device__ __forceinline__ float rbf(float f) { return __uint_as_float((unsigned)f2bf(f) << 16); }
__device__ __forceinline__ unsigned pack2(float a, float b) { return (unsigned)f2bf(a) | ((unsigned)f2bf(b) << 16); }
__device__ __forceinline__ unsigned clampw(float f) { const unsigned u = __float_as_uint(f); return u < 0xFFFFFFFEu ? u : 0xFFFFFFFEu; }
__device__ __forceinline__ v4u clamp4(v4f v) { return (v4u){clampw(v[0]), clampw(v[1]), clampw(v[2]), clampw(v[3])}; }
__device__ __forceinline__ unsigned umax4(v4u v) { return max(max(v[0], v[1]), max(v[2], v[3])); }

template <int CTRL> __device__ __forceinline__ float dpp(float v) {
    return __builtin_bit_cast(float, __builtin_amdgcn_update_dpp(0, __builtin_bit_cast(int, v), CTRL, 0xf, 0xf, true));
}
constexpr int ROR1 = 0x121, ROR2 = 0x122, ROR4 = 0x124, ROR8 = 0x128;
__device__ __forceinline__ float row_sum(float v) { v += dpp<ROR8>(v); v += dpp<ROR4>(v); v += dpp<ROR2>(v); v += dpp<ROR1>(v); return v; }
__device__ __forceinline__ float row_max(float v) { v = fmaxf(v, dpp<ROR8>(v)); v = fmaxf(v, dpp<ROR4>(v)); v = fmaxf(v, dpp<ROR2>(v)); v = fmaxf(v, dpp<ROR1>(v)); return v; }
__device__ __forceinline__ float swap16_add(float a, float b) {
    const auto r = __builtin_amdgcn_permlane16_swap(__float_as_uint(a), __float_as_uint(b), false, false);
    return __uint_as_float(r[0]) + __uint_as_float(r[1]);
}
__device__ __forceinline__ float swap32_add(float a, float b) {
    const auto r = __builtin_amdgcn_permlane32_swap(__float_as_uint(a), __float_as_uint(b), false, false);
    return __uint_as_float(r[0]) + __uint_as_float(r[1]);
}
__device__ __forceinline__ float x16_max(float a) {
    const auto r = __builtin_amdgcn_permlane16_swap(__float_as_uint(a), __float_as_uint(a), false, false);
    return fmaxf(__uint_as_float(r[0]), __uint_as_float(r[1]));
}
__device__ __forceinline__ float x32_max(float a) {
    const auto r = __builtin_amdgcn_permlane32_swap(__float_as_uint(a), __float_as_uint(a), false, false);
    return fmaxf(__uint_as_float(r[0]), __uint_as_float(r[1]));
}
__device__ __forceinline__ float wave_sum(float v) { v = row_sum(v); v = swap16_add(v, v); return swap32_add(v, v); }
__device__ __forceinline__ float wave_max(float v) { return x32_max(x16_max(row_max(v))); }
__device__ __forceinline__ float rows_reduce4(v4f v) { return swap32_add(swap16_add(v[0], v[1]), swap16_add(v[2], v[3])); }

__device__ __forceinline__ v4f mfma(v4u a, v4u b, v4f c) {
    return __builtin_amdgcn_mfma_f32_16x16x32_bf16(__builtin_bit_cast(v8bf, a), __builtin_bit_cast(v8bf, b), c, 0, 0, 0);
}
__device__ __forceinline__ v2u trread(LAS unsigned char* p) {
    return __builtin_bit_cast(v2u, __builtin_amdgcn_ds_read_tr16_b64_v4i16((LAS v4s*)p));
}
__device__ __forceinline__ v4u trfrag(LAS unsigned char* p, int hi_off) { const v2u a = trread(p), b = trread(p + hi_off); return (v4u){a[0], a[1], b[0], b[1]}; }

#define CBAR() asm volatile("" ::: "memory")
#define LGKM0() asm volatile("s_waitcnt lgkmcnt(0)" ::: "memory")
#define VM0() asm volatile("s_waitcnt vmcnt(0)" ::: "memory")
template <int N> __device__ __forceinline__ void vmwait() { asm volatile("s_waitcnt vmcnt(%0)" ::"n"(N) : "memory"); }

__device__ __forceinline__ void dma4(const void* sbase, unsigned dst, unsigned v0, unsigned v1, unsigned v2, unsigned v3) {
    unsigned keep;
    asm volatile("s_mov_b32 %0, m0\n\ts_mov_b32 m0, %2\n\ts_nop 0\n\t"
                 "global_load_lds_dwordx4 %3, %1 nt\n\tglobal_load_lds_dwordx4 %4, %1 offset:1024 nt\n\t"
                 "global_load_lds_dwordx4 %5, %1 offset:2048 nt\n\tglobal_load_lds_dwordx4 %6, %1 offset:3072 nt\n\ts_mov_b32 m0, %0"
                 : "=&s"(keep) : "s"(sbase), "s"(dst), "v"(v0), "v"(v1), "v"(v2), "v"(v3) : "memory");
}
__device__ __forceinline__ unsigned lds_ld(unsigned addr) {
    unsigned v;
    asm volatile("ds_read_b32 %0, %1\n\ts_waitcnt lgkmcnt(0)" : "=&v"(v) : "v"(addr) : "memory");
    return __builtin_amdgcn_readfirstlane(v);
}
__device__ __forceinline__ void lds_st(unsigned addr, unsigned v) { asm volatile("ds_write_b32 %0, %1" ::"v"(addr), "v"(v) : "memory"); }
__device__ __forceinline__ void lds_inc(unsigned addr, int lane) { if (lane == 0) asm volatile("ds_add_u32 %0, %1" ::"v"(addr), "v"(1u) : "memory"); }

__device__ __forceinline__ bool wall(bool p) { return __builtin_amdgcn_ballot_w64(!p) == 0ull; }
__device__ __forceinline__ float gelu_tanh(float x) {
    const float inner = 0.7978845608028654f * (x + 0.044715f * x * x * x);
    return 0.5f * x * (1.0f + tanhf(inner));
}

#define ST(i) do {} while (0)

__global__ void __launch_bounds__(512, 2) fused(const unsigned short* __restrict__ x, const unsigned short* __restrict__ Kc, const unsigned short* __restrict__ Vc,
                                                const unsigned short* __restrict__ wq, const unsigned short* __restrict__ wo, const unsigned short* __restrict__ wg,
                                                const unsigned short* __restrict__ wu, const unsigned short* __restrict__ wd, unsigned short* __restrict__ out,
                                                unsigned char* __restrict__ ws) {
    extern __shared__ __attribute__((aligned(16))) unsigned char smem[];
    LAS unsigned char* L8 = (LAS unsigned char*)smem;
    const unsigned lbase = (unsigned)(uintptr_t)L8;
    const int lane = threadIdx.x & 63, wave = __builtin_amdgcn_readfirstlane(threadIdx.x >> 6), b = blockIdx.x;
    const int c = lane & 15, lg = lane >> 4;
    const unsigned ctl = lbase + L_CTL;
    const __amdgpu_buffer_rsrc_t rs = __builtin_amdgcn_make_buffer_rsrc(ws, 0, WS_END, 0x00020000);
    const float LOG2E = 1.4426950408889634f;
    unsigned bud = SPIN;
    auto wait_ge = [&](int word, unsigned need) -> unsigned {
        unsigned v, t;
        bud = __builtin_amdgcn_readfirstlane(bud); need = __builtin_amdgcn_readfirstlane(need);
        asm volatile(".Lw%=:\n\tds_read_b32 %1, %3\n\ts_waitcnt lgkmcnt(0)\n\tv_readfirstlane_b32 %0, %1\n\ts_cmp_ge_u32 %0, %4\n\ts_cbranch_scc1 .Ld%=\n\t"
                     "s_sleep 1\n\ts_sub_u32 %2, %2, 1\n\ts_cbranch_scc0 .Lw%=\n\ts_mov_b32 %2, 0\n.Ld%=:"
                     : "=&s"(v), "=&v"(t), "+s"(bud) : "v"(ctl + word * 4), "s"(need) : "memory", "scc");
        return v;
    };
    auto spent = [&]() -> bool { const bool d = bud < 8u; bud = d ? 0u : bud - 8u; return d; };
    auto ld16 = [&](unsigned off) -> v4u { return __builtin_amdgcn_raw_buffer_load_b128(rs, off, 0, 16); };
    auto ld8 = [&](unsigned off) -> v2u { return __builtin_amdgcn_raw_buffer_load_b64(rs, off, 0, 16); };
    auto st16 = [&](unsigned off, v4u v) { __builtin_amdgcn_raw_buffer_store_b128(v, rs, off, 0, 16); };
    auto st8 = [&](unsigned off, v2u v) { __builtin_amdgcn_raw_buffer_store_b64(v, rs, off, 0, 16); };

    if (wave == 0) {
        ST(0);
        int gi = 0;
        int pub = 0;
        auto publish = [&](int units) { if (units > pub) { pub = units; lds_st(ctl + C_FULL * 4, (unsigned)units); } };
        auto drain = [&]() {
            vmwait<24>(); publish(gi / 2 - 3); vmwait<16>(); publish(gi / 2 - 2); vmwait<8>(); publish(gi / 2 - 1); vmwait<0>(); publish(gi / 2);
        };
        unsigned seen = 0;
        auto space = [&](int f) {
            if (f < NSLOT) return;
            const int p = f - NSLOT;
            if (p < 4) {
                const int wd_ = p < 2 ? C_WQ : C_WO;
                if (lds_ld(ctl + wd_ * 4) < 4u) { drain(); wait_ge(wd_, 4u); }
            } else if (seen < (unsigned)(p - 3)) {
                seen = min(lds_ld(ctl + C_DONE0 * 4), lds_ld(ctl + C_DONE1 * 4));
                if (seen < (unsigned)(p - 3)) { drain(); seen = min(wait_ge(C_DONE0, p - 3), wait_ge(C_DONE1, p - 3)); }
            }
        };
#define GROUP(W, SB, DST, V0, V1, V2, V3) do { vmwait<(W) - 4>(); publish((4 * gi - (W) + 4) >> 3); dma4(SB, DST, V0, V1, V2, V3); ++gi; } while (0)
        const unsigned r8 = lane >> 3, ch8 = ((lane & 7) ^ (((r8 >> 1) & 3) << 1)) << 4;
        {
            const unsigned char* base = (const unsigned char*)wq + (size_t)(b >> 6) * 256 * 8192 + (b & 63) * 128;
            const unsigned v = r8 * 8192 + ch8, d = 8 * 8192 - 1024;
#pragma unroll
            for (int g = 0; g < 4; ++g) { dma4(base + (size_t)g * 32 * 8192, lbase + g * 4096, v, v + d, v + 2 * d, v + 3 * d); ++gi; }
            __builtin_amdgcn_s_barrier();
#pragma unroll
            for (int g = 4; g < 8; ++g) GROUP(W_START, base + (size_t)g * 32 * 8192, lbase + g * 4096, v, v + d, v + 2 * d, v + 3 * d);
        }
        const unsigned vv0 = lg * 256 + ((c ^ (lg << 1)) << 4), vv1 = lg * 256 + ((c ^ ((4 + lg) << 1)) << 4);
        {
            const unsigned char* base = (const unsigned char*)wo + (size_t)(b >> 3) * 128 * 2048 + (b & 7) * 256;
            const unsigned a0 = vv0 + lg * (2048 - 256), a1 = vv1 + lg * (2048 - 256), d = 4 * 2048 - 1024;
#pragma unroll
            for (int g = 0; g < 8; ++g) GROUP(W_START, base + (size_t)g * 16 * 2048, lbase + 2 * SLOT + g * 4096, a0, a1 + d, a0 + 2 * d, a1 + 3 * d);
        }
        {
            const unsigned k0 = lg * 256 + ((c ^ lg) << 4), k1 = lg * 256 + ((c ^ (4 + lg)) << 4), k2 = lg * 256 + ((c ^ (8 + lg)) << 4), k3 = lg * 256 + ((c ^ (12 + lg)) << 4);
            const unsigned char* kp = (const unsigned char*)Kc + (size_t)b * NT * 8192;
            const unsigned char* vp = (const unsigned char*)Vc + (size_t)b * NT * 8192;
            int slot = 4;
#define TILE(W) do { space(4 + t); const unsigned dst = lbase + slot * SLOT; \
                GROUP(W, kp, dst, k0, k1, k2, k3); GROUP(W, kp + 4096, dst + 4096, k0, k1, k2, k3); \
                GROUP(W, vp, dst + 8192, vv0, vv1, vv0, vv1); GROUP(W, vp + 4096, dst + 12288, vv0, vv1, vv0, vv1); \
                kp += 8192; vp += 8192; slot = slot == NSLOT - 1 ? 0 : slot + 1; } while (0)
            int t = 0;
#pragma unroll 1
            for (; t < N_START; ++t) TILE(W_START);
#pragma unroll 1
            for (; t < NT; ++t) TILE(W_MAIN);
        }
        ST(1);
        if (b < NFFN) {
            vmwait<24>(); publish(gi / 2 - 3); vmwait<16>(); publish(gi / 2 - 2);
            const int rb = b & 3, cb = b >> 2;
            int slot = (4 + NT) % NSLOT;
            {
                const size_t o = (size_t)rb * 256 * 7168 + cb * 128;
                const unsigned char* gp = (const unsigned char*)wg + o;
                const unsigned char* up = (const unsigned char*)wu + o;
                const unsigned v = r8 * 7168 + ch8, d = 8 * 7168 - 1024;
#pragma unroll 1
                for (int i = 0; i < 4; ++i) {
                    space(4 + NT + i);
                    const unsigned dst = lbase + slot * SLOT;
                    GROUP(W_TAIL, gp, dst, v, v + d, v + 2 * d, v + 3 * d); GROUP(W_TAIL, gp + 32 * 7168, dst + 4096, v, v + d, v + 2 * d, v + 3 * d);
                    GROUP(W_TAIL, up, dst + 8192, v, v + d, v + 2 * d, v + 3 * d); GROUP(W_TAIL, up + 32 * 7168, dst + 12288, v, v + d, v + 2 * d, v + 3 * d);
                    gp += 64 * 7168; up += 64 * 7168; slot = slot == NSLOT - 1 ? 0 : slot + 1;
                }
            }
            {
                const unsigned char* dp = (const unsigned char*)wd + (size_t)cb * 64 * 2048 + rb * 512;
                const unsigned v = r8 * 2048 + ch8, d = 8 * 2048 - 1024;
#pragma unroll 1
                for (int i = 0; i < 2; ++i) {
                    space(4 + NT + 4 + i);
                    const unsigned dst = lbase + slot * SLOT;
                    GROUP(W_TAIL, dp, dst, v, v + d, v + 2 * d, v + 3 * d); GROUP(W_TAIL, dp + 32 * 2048, dst + 4096, v, v + d, v + 2 * d, v + 3 * d);
                    GROUP(W_TAIL, dp + 128, dst + 8192, v, v + d, v + 2 * d, v + 3 * d); GROUP(W_TAIL, dp + 128 + 32 * 2048, dst + 12288, v, v + d, v + 2 * d, v + 3 * d);
                    dp += 256; slot = slot == NSLOT - 1 ? 0 : slot + 1;
                }
            }
        }
        drain();
        ST(2);
    } else if (wave <= 2) {
        const int hf = wave - 1;
        __builtin_amdgcn_s_barrier();
        unsigned ka[4], va[4];
#pragma unroll
        for (int i = 0; i < 4; ++i) {
            ka[i] = c * 256 + (((4 * i + lg) ^ c) << 4);
            const int row = 4 * lg + (c >> 2);
            va[i] = 8192 + row * 256 + (((4 * hf + i) ^ (row & 7)) << 5) + 8 * (c & 3);
        }
        wait_ge(C_QRDY, 1u);
        v4u qf[4];
#pragma unroll
        for (int i = 0; i < 4; ++i) qf[i] = *(LAS v4u*)(L8 + L_QI + (c & 7) * 272 + (4 * i + lg) * 16);
        if (hf == 0) ST(3);
        float m = -INFINITY, l = 0.f;
        v4f acc[4];
#pragma unroll
        for (int i = 0; i < 4; ++i) acc[i] = (v4f){0, 0, 0, 0};
        const float scale = 0.08838834764831845f;
        unsigned full = 0;
        int slot = 4;
#pragma unroll 1
        for (int t = 0; t < NT; ++t) {
            LAS unsigned char* sp = L8 + slot * SLOT;
            if (full < (unsigned)(9 + 2 * t)) full = wait_ge(C_FULL, 9 + 2 * t);
            CBAR();
            v4u kf[8];
#pragma unroll
            for (int i = 0; i < 4; ++i) { kf[i] = *(LAS v4u*)(sp + ka[i]); kf[4 + i] = *(LAS v4u*)(sp + ka[i] + 4096); }
            v4f s0 = {0, 0, 0, 0}, s1 = {0, 0, 0, 0};
#pragma unroll
            for (int i = 0; i < 4; ++i) { s0 = mfma(kf[i], qf[i], s0); s1 = mfma(kf[4 + i], qf[i], s1); }
            float s[8];
#pragma unroll
            for (int j = 0; j < 4; ++j) { s[j] = rbf(rbf(s0[j]) * scale); s[4 + j] = rbf(rbf(s1[j]) * scale); }
            float mx = fmaxf(fmaxf(fmaxf(s[0], s[1]), fmaxf(s[2], s[3])), fmaxf(fmaxf(s[4], s[5]), fmaxf(s[6], s[7])));
            mx = x32_max(x16_max(mx));
            const float mn = fmaxf(m, mx);
            const float alpha = __builtin_amdgcn_exp2f((m - mn) * LOG2E);
            m = mn;
            float p[8], ps = 0.f;
#pragma unroll
            for (int j = 0; j < 8; ++j) { p[j] = __builtin_amdgcn_exp2f((s[j] - mn) * LOG2E); ps += p[j]; }
            l = l * alpha + ps;
            v4u pf;
#pragma unroll
            for (int j = 0; j < 4; ++j) pf[j] = pack2(p[2 * j], p[2 * j + 1]);
#pragma unroll
            for (int i = 0; i < 4; ++i) acc[i] *= alpha;
            if (full < (unsigned)(10 + 2 * t)) full = wait_ge(C_FULL, 10 + 2 * t);
            CBAR();
            v4u vf[4];
#pragma unroll
            for (int i = 0; i < 4; ++i) vf[i] = trfrag(sp + va[i], 4096);
            asm volatile("s_waitcnt lgkmcnt(0)" : "+v"(vf[0]), "+v"(vf[1]), "+v"(vf[2]), "+v"(vf[3])::"memory");
            lds_st(ctl + (C_DONE0 + hf) * 4, (unsigned)(t + 1));
#pragma unroll
            for (int i = 0; i < 4; ++i) acc[i] = mfma(vf[i], pf, acc[i]);
            slot = slot == NSLOT - 1 ? 0 : slot + 1;
        }
        const unsigned o = WS_ACC + ((unsigned)((b >> 6) * 8 + (c & 7)) * 64 + (b & 63)) * 512 + hf * 256 + (c >> 3) * 64 + lg * 16;
        const bool up = c >= 8;
        st16(o, clamp4(up ? acc[1] : acc[0]));
        st16(o + 128, clamp4(up ? acc[3] : acc[2]));
        if (hf == 0) {
            l = swap16_add(l, l); l = swap32_add(l, l);
            if (lane < 8) st8(WS_ML + b * 64 + lane * 8, (v2u){clampw(m), clampw(l)});
        }
        lds_inc(ctl + C_APUB * 4, lane);
        ST(4 + hf);
    } else if (wave == 3) {
        unsigned vz;
        asm volatile("v_mov_b32 %0, 0" : "=v"(vz));
        const v2u epw = ld8(WS_EPOCH + vz);
        if (lane < 32) *(LAS unsigned*)(L8 + L_CTL + lane * 4) = 0u;
        *(LAS v2u*)(L8 + L_ONE + lane * 8) = (v2u){0x3F803F80u, 0x3F803F80u};
        LGKM0();
        __builtin_amdgcn_s_barrier();
        const v4u sent = {SENT, SENT, SENT, SENT};
#pragma unroll
        for (int i = 0; i < 4; ++i) st16(WS_ACC + ((unsigned)((b >> 6) * 8 + 2 * i + (lane >> 5)) * 64 + (b & 63)) * 512 + (lane & 31) * 16, sent);
        if (lane < 4) st16(WS_ML + b * 64 + lane * 16, sent);
        if (lane < 32) st16(WS_P + (b >> 3) * 4096 + (b & 7) * 512 + lane * 16, sent);
        if (b < NFFN) {
            st16(WS_X + b * 1280 + lane * 16, sent);
            if (lane < 16) st16(WS_X + b * 1280 + 1024 + lane * 16, sent);
            st16(WS_Y + (b >> 2) * 4096 + (b & 3) * 1024 + lane * 16, sent);
        }
        const u64 ep = (u64)epw[0] | ((u64)epw[1] << 32);
        u64 tag = ep + 0x9E3779B97F4A7C15ull;
        tag = (tag ^ (tag >> 30)) * 0xBF58476D1CE4E5B9ull; tag = (tag ^ (tag >> 27)) * 0x94D049BB133111EBull; tag ^= tag >> 31;
        tag += (tag == ep);
        VM0();
        wait_ge(C_QPUB, 4u);
        if (lane == 0) __hip_atomic_store((GAS u64*)(ws + WS_FLAG + b * 128), tag, RLX_AGENT);
        ST(6);
        const int g = b >> 6;
        {
            GAS u64* fp = (GAS u64*)(ws + WS_FLAG) + ((lane >> 4) * 64 + g * 16 + c) * 16;
            #pragma nounroll
            for (;;) { const u64 v = __hip_atomic_load(fp, RLX_AGENT); if (wall(v == tag) || spent()) break; }
        }
        ST(7);
        {
            v4u q[16];
#pragma unroll
            for (int i = 0; i < 16; ++i) q[i] = ld16(WS_QP + (i >> 2) * 16384 + g * 4096 + (i & 3) * 1024 + lane * 16);
#pragma unroll
            for (int i = 0; i < 4; ++i) {
                const v4f s = (__builtin_bit_cast(v4f, q[i]) + __builtin_bit_cast(v4f, q[4 + i])) + (__builtin_bit_cast(v4f, q[8 + i]) + __builtin_bit_cast(v4f, q[12 + i]));
                *(LAS v2u*)(L8 + L_QI + (2 * i + (lane >> 5)) * 272 + (lane & 31) * 8) = (v2u){pack2(s[0], s[1]), pack2(s[2], s[3])};
            }
            LGKM0();
            lds_st(ctl + C_QRDY * 4, 1u);
        }
        {
            GAS u64* fp = (GAS u64*)(ws + WS_FLAG) + lane * 16;
#pragma nounroll
            for (;;) {
                const u64 v0 = __hip_atomic_load(fp, RLX_AGENT), v1 = __hip_atomic_load(fp + 1024, RLX_AGENT), v2 = __hip_atomic_load(fp + 2048, RLX_AGENT), v3 = __hip_atomic_load(fp + 3072, RLX_AGENT);
                if (wall(v0 == tag && v1 == tag && v2 == tag && v3 == tag) || spent()) break;
                __builtin_amdgcn_s_sleep(8);
            }
            lds_st(ctl + C_ALLF * 4, 1u);
            if (b == 0 && lane == 0) __hip_atomic_store((GAS u64*)(ws + WS_EPOCH), tag, RLX_AGENT);
        }
        if (b >= NFFN) {
            const int a = b - NFFN, col = a * 32 + 4 * (lane & 7) + lg;
            const float xv = __uint_as_float((unsigned)x[col] << 16);
            wait_ge(C_OS, 4u);
            v4u pv[4];
#pragma nounroll
            for (;;) {
                CBAR();
                unsigned mx = 0;
#pragma unroll
                for (int j = 0; j < 4; ++j) pv[j] = ld16(WS_P + (8 * j + (lane >> 3)) * 4096 + a * 128 + (lane & 7) * 16);
#pragma unroll
                for (int j = 0; j < 4; ++j) mx = max(mx, umax4(pv[j]));
                if (wall(mx != SENT) || spent()) break;
            }
            float hs;
            {
                v4f e = (__builtin_bit_cast(v4f, pv[0]) + __builtin_bit_cast(v4f, pv[1])) + (__builtin_bit_cast(v4f, pv[2]) + __builtin_bit_cast(v4f, pv[3]));
                hs = rows_reduce4(e); hs += dpp<ROR8>(hs);
            }
            const float h = rbf(xv + rbf(hs));
            ST(14);
            v4u yv[7];
#pragma nounroll
            for (;;) {
                CBAR();
                unsigned mx = 0;
#pragma unroll
                for (int j = 0; j < 7; ++j) yv[j] = ld16(WS_Y + (8 * j + (lane >> 3)) * 4096 + a * 128 + (lane & 7) * 16);
#pragma unroll
                for (int j = 0; j < 7; ++j) mx = max(mx, umax4(yv[j]));
                if (wall(mx != SENT) || spent()) break;
            }
            v4f e = __builtin_bit_cast(v4f, yv[0]);
#pragma unroll
            for (int j = 1; j < 7; ++j) e += __builtin_bit_cast(v4f, yv[j]);
            float ys = rows_reduce4(e); ys += dpp<ROR8>(ys);
            if (c < 8) out[col] = f2bf(h + rbf(ys));
            ST(15);
        }
    } else {
        const int w = wave - 4;
        const int kpos = (lane & 32) | ((lane & 12) << 1) | ((lane & 16) >> 2) | (lane & 3);
        float xres;
        {
            const v4u xa = *(const GAS v4u*)(x + lane * 16), xb = *(const GAS v4u*)(x + lane * 16 + 8);
            const float xm = __uint_as_float((unsigned)x[(b >> 6) * 256 + w * 64 + lane] << 16);
            xres = __uint_as_float((unsigned)x[(b & 3) * 256 + w * 64 + 4 * c + lg] << 16);
            __builtin_amdgcn_s_barrier();
            float v[16];
#pragma unroll
            for (int j = 0; j < 4; ++j) { v[2 * j] = bflo(xa[j]); v[2 * j + 1] = bfhi(xa[j]); v[8 + 2 * j] = bflo(xb[j]); v[9 + 2 * j] = bfhi(xb[j]); }
            float s = 0.f;
#pragma unroll
            for (int j = 0; j < 16; ++j) s += v[j];
            const float mean = wave_sum(s) * (1.0f / 1024);
            float q = 0.f;
#pragma unroll
            for (int j = 0; j < 16; ++j) { const float d = v[j] - mean; q += d * d; }
            const float var = wave_sum(q) * (1.0f / 1024);
            const float sd = rbf(sqrtf(rbf(rbf(var) + 1e-5f)));
            *(LAS unsigned short*)(L8 + L_XS + (w * 64 + kpos) * 2) = f2bf(rbf(xm - rbf(mean)) / sd);
            LGKM0();
            lds_inc(ctl + C_XS * 4, lane);
        }
        const int trow = 4 * lg + (c >> 2);
        const unsigned ta128 = trow * 128 + 8 * (c & 3), tx128 = (trow >> 1) & 3;
        {
            wait_ge(C_XS, 4u);
            v4u xf[8];
#pragma unroll
            for (int k = 0; k < 8; ++k) xf[k] = *(LAS v4u*)(L8 + L_XS + k * 64 + lg * 16);
            wait_ge(C_FULL, 4u);
            v4u wf[8];
#pragma unroll
            for (int k = 0; k < 8; ++k) wf[k] = trfrag(L8 + k * 4096 + ta128 + ((w ^ tx128) << 5), 2048);
            v4f d0 = {0, 0, 0, 0}, d1 = {0, 0, 0, 0};
#pragma unroll
            for (int k = 0; k < 8; k += 2) { d0 = mfma(wf[k], xf[k], d0); d1 = mfma(wf[k + 1], xf[k + 1], d1); }
            d0 += d1;
            if (c == 0) st16(WS_QP + (b >> 6) * 16384 + ((b & 63) * 64 + w * 16 + lg * 4) * 4, __builtin_bit_cast(v4u, d0));
            lds_inc(ctl + C_WQ * 4, lane);
            VM0();
            lds_inc(ctl + C_QPUB * 4, lane);
            if (w == 0) ST(8);
        }
        v4u wof[8];
        {
            wait_ge(C_FULL, 8u);
#pragma unroll
            for (int k = 0; k < 4; ++k)
#pragma unroll
                for (int t = 0; t < 2; ++t) wof[2 * k + t] = trfrag(L8 + 2 * SLOT + k * 8192 + trow * 256 + (((2 * w + t) ^ (trow & 7)) << 5) + 8 * (c & 3), 4096);
            asm volatile("s_waitcnt lgkmcnt(0)" : "+v"(wof[0]), "+v"(wof[1]), "+v"(wof[2]), "+v"(wof[3]), "+v"(wof[4]), "+v"(wof[5]), "+v"(wof[6]), "+v"(wof[7])::"memory");
            lds_inc(ctl + C_WO * 4, lane);
        }
        wait_ge(C_ALLF, 1u);
        wait_ge(C_APUB, 2u);
        {
            const int a = b >> 3;
            v4u av[8]; v2u ml;
#pragma nounroll
            for (;;) {
                CBAR();
                ml = ld8(WS_ML + ((b >> 6) * 64 + lane) * 64 + (a & 7) * 8);
#pragma unroll
                for (int j = 0; j < 8; ++j) av[j] = ld16(WS_ACC + (a * 64 + 8 * (lane >> 3) + j) * 512 + w * 128 + (lane & 7) * 16);
                unsigned mx = max(ml[0], ml[1]);
#pragma unroll
                for (int j = 0; j < 8; ++j) mx = max(mx, umax4(av[j]));
                if (wall(mx != SENT) || spent()) break;
            }
            if (w == 0) ST(9);
            const float ms = __uint_as_float(ml[0]);
            const float M = wave_max(ms);
            const float f = __builtin_amdgcn_exp2f((ms - M) * LOG2E);
            *(LAS float*)(L8 + L_WS + w * 256 + lane * 4) = f;
            CBAR();
            const v4f f0 = *(LAS v4f*)(L8 + L_WS + w * 256 + (lane >> 3) * 32), f1 = *(LAS v4f*)(L8 + L_WS + w * 256 + (lane >> 3) * 32 + 16);
            const float L = wave_sum(f * __uint_as_float(ml[1]));
            v4f e = __builtin_bit_cast(v4f, av[0]) * f0[0];
#pragma unroll
            for (int j = 1; j < 8; ++j) e += __builtin_bit_cast(v4f, av[j]) * (j < 4 ? f0[j & 3] : f1[j & 3]);
            float os = rows_reduce4(e); os += dpp<ROR8>(os);
            const int k = 4 * (lane & 7) + lg;
            if (c < 8) *(LAS unsigned short*)(L8 + L_OS + (w * 32 + (((k & 12) << 1) | ((k & 16) >> 2) | (k & 3))) * 2) = f2bf(os / L);
            LGKM0();
            lds_inc(ctl + C_OS * 4, lane);
            wait_ge(C_OS, 4u);
            v4u of[4];
#pragma unroll
            for (int k2 = 0; k2 < 4; ++k2) of[k2] = *(LAS v4u*)(L8 + L_OS + k2 * 64 + lg * 16);
            v4f d0 = {0, 0, 0, 0}, d1 = {0, 0, 0, 0};
#pragma unroll
            for (int k2 = 0; k2 < 4; ++k2) { d0 = mfma(wof[2 * k2], of[k2], d0); d1 = mfma(wof[2 * k2 + 1], of[k2], d1); }
            if (c < 2) st16(WS_P + a * 4096 + (b & 7) * 512 + w * 128 + c * 64 + lg * 16, clamp4(c ? d1 : d0));
            if (w == 0) ST(10);
        }
        if (b < NFFN) {
            const int rb = b & 3, cb = b >> 2;
            constexpr int F0 = 4 + NT;
            {
                v4u pv[8];
#pragma nounroll
                for (;;) {
                    CBAR();
#pragma unroll
                    for (int j = 0; j < 8; ++j) pv[j] = ld16(WS_P + (8 * (j >> 1) + 2 * lg + (j & 1)) * 4096 + rb * 1024 + w * 256 + c * 16);
                    unsigned mx = 0;
#pragma unroll
                    for (int j = 0; j < 8; ++j) mx = max(mx, umax4(pv[j]));
                    if (wall(mx != SENT) || spent()) break;
                }
                if (w == 0) ST(11);
                const v4f e0 = (__builtin_bit_cast(v4f, pv[0]) + __builtin_bit_cast(v4f, pv[2])) + (__builtin_bit_cast(v4f, pv[4]) + __builtin_bit_cast(v4f, pv[6]));
                const v4f e1 = (__builtin_bit_cast(v4f, pv[1]) + __builtin_bit_cast(v4f, pv[3])) + (__builtin_bit_cast(v4f, pv[5]) + __builtin_bit_cast(v4f, pv[7]));
                const float hs = rows_reduce4(e0) + rows_reduce4(e1);
                const float h = rbf(xres + rbf(hs));
                const int k = 4 * c + lg;
                *(LAS unsigned short*)(L8 + L_HS + (w * 64 + ((k & 32) | ((k & 12) << 1) | ((k & 16) >> 2) | (k & 3))) * 2) = f2bf(h);
                LGKM0();
                lds_inc(ctl + C_HS * 4, lane);
                const float hsum = wave_sum(h);
                const float dd = h - hsum * (1.0f / 64);
                const float hm2 = wave_sum(dd * dd);
                wait_ge(C_FULL, 2 * (F0 + 4));
                v4u gf[8], uf[8];
#pragma unroll
                for (int k2 = 0; k2 < 8; ++k2) {
                    LAS unsigned char* sp = L8 + ((F0 + (k2 >> 1)) % NSLOT) * SLOT + (k2 & 1) * 4096 + ta128 + ((w ^ tx128) << 5);
                    gf[k2] = trfrag(sp, 2048); uf[k2] = trfrag(sp + 8192, 2048);
                }
                wait_ge(C_HS, 4u);
                v4u hf[8];
#pragma unroll
                for (int k2 = 0; k2 < 8; ++k2) hf[k2] = *(LAS v4u*)(L8 + (c < 8 ? L_HS : L_ONE) + k2 * 64 + lg * 16);
                v4f dg = {0, 0, 0, 0}, du = {0, 0, 0, 0};
#pragma unroll
                for (int k2 = 0; k2 < 8; ++k2) { dg = mfma(gf[k2], hf[k2], dg); du = mfma(uf[k2], hf[k2], du); }
                const float gs = (c & 2) ? ((c & 1) ? dg[3] : dg[2]) : ((c & 1) ? dg[1] : dg[0]);
                const float us = (c & 2) ? ((c & 1) ? du[3] : du[2]) : ((c & 1) ? du[1] : du[0]);
                v4f rec = {gs, us, dpp<ROR8>(gs), dpp<ROR8>(us)};
                if (lane == 4) rec = (v4f){hsum, hm2, 0.f, 0.f};
                if (c < 4 || lane == 4) st16(WS_X + b * 1280 + w * 320 + (lane == 4 ? 16 : 4 * lg + c) * 16, clamp4(rec));
                if (w == 0) ST(12);
            }
            {
                wait_ge(C_FULL, 2 * (F0 + 4) + w + 1);
                v4u df[8];
#pragma unroll
                for (int k2 = 0; k2 < 2; ++k2)
#pragma unroll
                    for (int t = 0; t < 4; ++t)
                        df[4 * k2 + t] = trfrag(L8 + ((F0 + 4 + (w >> 1)) % NSLOT) * SLOT + (w & 1) * 8192 + k2 * 4096 + ta128 + ((t ^ tx128) << 5), 2048);
                v4u xv[4]; v2u sv;
#pragma nounroll
                for (;;) {
                    CBAR();
#pragma unroll
                    for (int j = 0; j < 4; ++j) xv[j] = ld16(WS_X + (cb * 4 + j) * 1280 + lg * 320 + c * 16);
                    sv = ld8(WS_X + cb * 5120 + c * 320 + 256);
                    unsigned mx = max(sv[0], sv[1]);
#pragma unroll
                    for (int j = 0; j < 4; ++j) mx = max(mx, umax4(xv[j]));
                    if (wall(mx != SENT) || spent()) break;
                }
                const float ssum = __uint_as_float(sv[0]);
                const float mean = row_sum(ssum) * (1.0f / 1024);
                const float dm = ssum * (1.0f / 64) - mean;
                const float var = row_sum(__uint_as_float(sv[1]) + 64.f * dm * dm) * (1.0f / 1024);
                const float mb = rbf(mean), sd = rbf(sqrtf(rbf(rbf(var) + 1e-5f)));
                const v4f r = (__builtin_bit_cast(v4f, xv[0]) + __builtin_bit_cast(v4f, xv[1])) + (__builtin_bit_cast(v4f, xv[2]) + __builtin_bit_cast(v4f, xv[3]));
                const float gg = rbf((r[0] - mb * r[2]) / sd), uu = rbf((r[1] - mb * r[3]) / sd);
                *(LAS unsigned short*)(L8 + L_AS + w * 128 + kpos * 2) = f2bf(rbf(gelu_tanh(gg)) * uu);
                CBAR();
                const v4u a0 = *(LAS v4u*)(L8 + L_AS + w * 128 + lg * 16), a1 = *(LAS v4u*)(L8 + L_AS + w * 128 + 64 + lg * 16);
                v4f d[4];
#pragma unroll
                for (int t = 0; t < 4; ++t) { d[t] = mfma(df[t], a0, (v4f){0, 0, 0, 0}); d[t] = mfma(df[4 + t], a1, d[t]); }
                const v4f y = (c & 2) ? ((c & 1) ? d[3] : d[2]) : ((c & 1) ? d[1] : d[0]);
                if (c < 4) st16(WS_Y + cb * 4096 + rb * 1024 + w * 256 + c * 64 + lg * 16, clamp4(y));
                if (w == 0) ST(13);
            }
        }
    }
}

extern "C" void kernel_launch(void* const* d_in, const int* in_sizes, int n_in, void* d_out, int out_size, void* d_ws, size_t ws_size, hipStream_t stream) {
    (void)in_sizes; (void)n_in; (void)out_size; (void)ws_size;
    (void)hipFuncSetAttribute((const void*)fused, hipFuncAttributeMaxDynamicSharedMemorySize, L_END);
    fused<<<NWG, 512, L_END, stream>>>((const unsigned short*)d_in[0], (const unsigned short*)d_in[1], (const unsigned short*)d_in[2], (const unsigned short*)d_in[3],
                                       (const unsigned short*)d_in[4], (const unsigned short*)d_in[6], (const unsigned short*)d_in[7], (const unsigned short*)d_in[8],
                                       (unsigned short*)d_out, (unsigned char*)d_ws);
}
```

```cpp
#define POL_W ""
#define POL_K " nt"
#define POL_V " nt"
#include <hip/hip_runtime.h>
#include <stdint.h>

typedef unsigned v4u __attribute__((ext_vector_type(4)));
typedef unsigned v2u __attribute__((ext_vector_type(2)));
typedef float v4f __attribute__((ext_vector_type(4)));
typedef __bf16 v8bf __attribute__((ext_vector_type(8)));
typedef short v4s __attribute__((ext_vector_type(4)));
typedef unsigned long long u64;
#define GAS __attribute__((address_space(1)))
#define LAS __attribute__((address_space(3)))
#define RLX_AGENT __ATOMIC_RELAXED, __HIP_MEMORY_SCOPE_AGENT

#define W_START 20
#define W_MAIN 32
#define W_TAIL 16
#define N_START 4

constexpr int NWG = 256, NFFN = 224, NT = 32;
constexpr int NSLOT = 9, SLOT = 16384;
constexpr unsigned WS_EPOCH = 0, WS_FLAG = 4096, WS_QP = WS_FLAG + 256 * 128, WS_ACC = WS_QP + 4 * 4096 * 4, WS_ML = WS_ACC + 32 * 64 * 512,
                   WS_P = WS_ML + 256 * 64, WS_X = WS_P + 32 * 4096, WS_Y = WS_X + 56 * 4 * 1280, WS_END = WS_Y + 56 * 4096, WS_STAMP = 4u << 20;
constexpr int L_QI = NSLOT * SLOT, L_XS = L_QI + 8 * 272, L_OS = L_XS + 512, L_HS = L_OS + 256, L_ONE = L_HS + 512, L_WS = L_ONE + 512, L_AS = L_WS + 1024,
              L_CTL = L_AS + 512, L_STAMP = L_CTL + 128, L_END = L_STAMP + 512;
enum { C_FULL = 0, C_DONE0, C_DONE1, C_WQ, C_WO, C_XS, C_QPUB, C_QRDY, C_ALLF, C_APUB, C_OS, C_HS };
static_assert(L_END <= 160 * 1024 && (L_XS & 15) == 0, "lds");
constexpr unsigned SENT = 0xFFFFFFFFu;
constexpr unsigned SPIN = 1u << 22;

__device__ __forceinline__ float bflo(unsigned u) { return __uint_as_float(u << 16); }
__device__ __forceinline__ float bfhi(unsigned u) { return __uint_as_float(u & 0xffff0000u); }
__device__ __forceinline__ unsigned short f2bf(float f) { __bf16 b = (__bf16)f; return __builtin_bit_cast(unsigned short, b); }
__device__ __forceinline__ float rbf(float f) { return __uint_as_float((unsigned)f2bf(f) << 16); }
__device__ __forceinline__ unsigned pack2(float a, float b) { return (unsigned)f2bf(a) | ((unsigned)f2bf(b) << 16); }
__device__ __forceinline__ unsigned clampw(float f) { const unsigned u = __float_as_uint(f); return u < 0xFFFFFFFEu ? u : 0xFFFFFFFEu; }
__device__ __forceinline__ v4u clamp4(v4f v) { return (v4u){clampw(v[0]), clampw(v[1]), clampw(v[2]), clampw(v[3])}; }
__device__ __forceinline__ unsigned umax4(v4u v) { return max(max(v[0], v[1]), max(v[2], v[3])); }

template <int CTRL> __device__ __forceinline__ float dpp(float v) {
    return __builtin_bit_cast(float, __builtin_amdgcn_update_dpp(0, __builtin_bit_cast(int, v), CTRL, 0xf, 0xf, true));
}
constexpr int ROR1 = 0x121, ROR2 = 0x122, ROR4 = 0x124, ROR8 = 0x128;
__device__ __forceinline__ float row_sum(float v) { v += dpp<ROR8>(v); v += dpp<ROR4>(v); v += dpp<ROR2>(v); v += dpp<ROR1>(v); return v; }
__device__ __forceinline__ float row_max(float v) { v = fmaxf(v, dpp<ROR8>(v)); v = fmaxf(v, dpp<ROR4>(v)); v = fmaxf(v, dpp<ROR2>(v)); v = fmaxf(v, dpp<ROR1>(v)); return v; }
__device__ __forceinline__ float swap16_add(float a, float b) {
    const auto r = __builtin_amdgcn_permlane16_swap(__float_as_uint(a), __float_as_uint(b), false, false);
    return __uint_as_float(r[0]) + __uint_as_float(r[1]);
}
__device__ __forceinline__ float swap32_add(float a, float b) {
    const auto r = __builtin_amdgcn_permlane32_swap(__float_as_uint(a), __float_as_uint(b), false, false);
    return __uint_as_float(r[0]) + __uint_as_float(r[1]);
}
__device__ __forceinline__ float x16_max(float a) {
    const auto r = __builtin_amdgcn_permlane16_swap(__float_as_uint(a), __float_as_uint(a), false, false);
    return fmaxf(__uint_as_float(r[0]), __uint_as_float(r[1]));
}
__device__ __forceinline__ float x32_max(float a) {
    const auto r = __builtin_amdgcn_permlane32_swap(__float_as_uint(a), __float_as_uint(a), false, false);
    return fmaxf(__uint_as_float(r[0]), __uint_as_float(r[1]));
}
__device__ __forceinline__ float wave_sum(float v) { v = row_sum(v); v = swap16_add(v, v); return swap32_add(v, v); }
__device__ __forceinline__ float wave_max(float v) { return x32_max(x16_max(row_max(v))); }
__device__ __forceinline__ float rows_reduce4(v4f v) { return swap32_add(swap16_add(v[0], v[1]), swap16_add(v[2], v[3])); }

__device__ __forceinline__ v4f mfma(v4u a, v4u b, v4f c) {
    return __builtin_amdgcn_mfma_f32_16x16x32_bf16(__builtin_bit_cast(v8bf, a), __builtin_bit_cast(v8bf, b), c, 0, 0, 0);
}
__device__ __forceinline__ v2u trread(LAS unsigned char* p) {
    return __builtin_bit_cast(v2u, __builtin_amdgcn_ds_read_tr16_b64_v4i16((LAS v4s*)p));
}
__device__ __forceinline__ v4u trfrag(LAS unsigned char* p, int hi_off) { const v2u a = trread(p), b = trread(p + hi_off); return (v4u){a[0], a[1], b[0], b[1]}; }

#define CBAR() asm volatile("" ::: "memory")
#define LGKM0() asm volatile("s_waitcnt lgkmcnt(0)" ::: "memory")
#define VM0() asm volatile("s_waitcnt vmcnt(0)" ::: "memory")
template <int N> __device__ __forceinline__ void vmwait() { asm volatile("s_waitcnt vmcnt(%0)" ::"n"(N) : "memory"); }

#define DMA4(NAME, POL) \
__device__ __forceinline__ void NAME(const void* sbase, unsigned dst, unsigned v0, unsigned v1, unsigned v2, unsigned v3) { \
    unsigned keep; \
    asm volatile("s_mov_b32 %0, m0\n\ts_mov_b32 m0, %2\n\ts_nop 0\n\t" \
                 "global_load_lds_dwordx4 %3, %1" POL "\n\tglobal_load_lds_dwordx4 %4, %1 offset:1024" POL "\n\t" \
                 "global_load_lds_dwordx4 %5, %1 offset:2048" POL "\n\tglobal_load_lds_dwordx4 %6, %1 offset:3072" POL "\n\ts_mov_b32 m0, %0" \
                 : "=&s"(keep) : "s"(sbase), "s"(dst), "v"(v0), "v"(v1), "v"(v2), "v"(v3) : "memory"); \
}
DMA4(dma4, POL_W)
DMA4(dma4k, POL_K)
DMA4(dma4v, POL_V)
__device__ __forceinline__ unsigned lds_ld(unsigned addr) {
    unsigned v;
    asm volatile("ds_read_b32 %0, %1\n\ts_waitcnt lgkmcnt(0)" : "=&v"(v) : "v"(addr) : "memory");
    return __builtin_amdgcn_readfirstlane(v);
}
__device__ __forceinline__ void lds_st(unsigned addr, unsigned v) { asm volatile("ds_write_b32 %0, %1" ::"v"(addr), "v"(v) : "memory"); }
__device__ __forceinline__ void lds_inc(unsigned addr, int lane) { if (lane == 0) asm volatile("ds_add_u32 %0, %1" ::"v"(addr), "v"(1u) : "memory"); }

__device__ __forceinline__ bool wall(bool p) { return __builtin_amdgcn_ballot_w64(!p) == 0ull; }
__device__ __forceinline__ float gelu_tanh(float x) {
    const float inner = 0.7978845608028654f * (x + 0.044715f * x * x * x);
    return 0.5f * x * (1.0f + tanhf(inner));
}

#define ST(i) do {} while (0)

__global__ void __launch_bounds__(512, 2) fused(const unsigned short* __restrict__ x, const unsigned short* __restrict__ Kc, const unsigned short* __restrict__ Vc,
                                                const unsigned short* __restrict__ wq, const unsigned short* __restrict__ wo, const unsigned short* __restrict__ wg,
                                                const unsigned short* __restrict__ wu, const unsigned short* __restrict__ wd, unsigned short* __restrict__ out,
                                                unsigned char* __restrict__ ws) {
    extern __shared__ __attribute__((aligned(16))) unsigned char smem[];
    LAS unsigned char* L8 = (LAS unsigned char*)smem;
    const unsigned lbase = (unsigned)(uintptr_t)L8;
    const int lane = threadIdx.x & 63, wave = __builtin_amdgcn_readfirstlane(threadIdx.x >> 6), b = blockIdx.x;
    const int c = lane & 15, lg = lane >> 4;
    const unsigned ctl = lbase + L_CTL;
    const __amdgpu_buffer_rsrc_t rs = __builtin_amdgcn_make_buffer_rsrc(ws, 0, WS_END, 0x00020000);
    const float LOG2E = 1.4426950408889634f;
    unsigned bud = SPIN;
    auto wait_ge = [&](int word, unsigned need) -> unsigned {
        unsigned v, t;
        bud = __builtin_amdgcn_readfirstlane(bud); need = __builtin_amdgcn_readfirstlane(need);
        asm volatile(".Lw%=:\n\tds_read_b32 %1, %3\n\ts_waitcnt lgkmcnt(0)\n\tv_readfirstlane_b32 %0, %1\n\ts_cmp_ge_u32 %0, %4\n\ts_cbranch_scc1 .Ld%=\n\t"
                     "s_sleep 1\n\ts_sub_u32 %2, %2, 1\n\ts_cbranch_scc0 .Lw%=\n\ts_mov_b32 %2, 0\n.Ld%=:"
                     : "=&s"(v), "=&v"(t), "+s"(bud) : "v"(ctl + word * 4), "s"(need) : "memory", "scc");
        return v;
    };
    auto spent = [&]() -> bool { const bool d = bud < 8u; bud = d ? 0u : bud - 8u; return d; };
    auto ld16 = [&](unsigned off) -> v4u { return __builtin_amdgcn_raw_buffer_load_b128(rs, off, 0, 16); };
    auto ld8 = [&](unsigned off) -> v2u { return __builtin_amdgcn_raw_buffer_load_b64(rs, off, 0, 16); };
    auto st16 = [&](unsigned off, v4u v) { __builtin_amdgcn_raw_buffer_store_b128(v, rs, off, 0, 16); };
    auto st8 = [&](unsigned off, v2u v) { __builtin_amdgcn_raw_buffer_store_b64(v, rs, off, 0, 16); };

    if (wave == 0) {
        ST(0);
        int gi = 0;
        int pub = 0;
        auto publish = [&](int units) { if (units > pub) { pub = units; lds_st(ctl + C_FULL * 4, (unsigned)units); } };
        auto drain = [&]() {
            vmwait<24>(); publish(gi / 2 - 3); vmwait<16>(); publish(gi / 2 - 2); vmwait<8>(); publish(gi / 2 - 1); vmwait<0>(); publish(gi / 2);
        };
        unsigned seen = 0;
        auto space = [&](int f) {
            if (f < NSLOT) return;
            const int p = f - NSLOT;
            if (p < 4) {
                const int wd_ = p < 2 ? C_WQ : C_WO;
                if (lds_ld(ctl + wd_ * 4) < 4u) { drain(); wait_ge(wd_, 4u); }
            } else if (seen < (unsigned)(p - 3)) {
                seen = min(lds_ld(ctl + C_DONE0 * 4), lds_ld(ctl + C_DONE1 * 4));
                if (seen < (unsigned)(p - 3)) { drain(); seen = min(wait_ge(C_DONE0, p - 3), wait_ge(C_DONE1, p - 3)); }
            }
        };
#define GROUPX(FN, W, SB, DST, V0, V1, V2, V3) do { vmwait<(W) - 4>(); publish((4 * gi - (W) + 4) >> 3); FN(SB, DST, V0, V1, V2, V3); ++gi; } while (0)
#define GROUP(...) GROUPX(dma4, __VA_ARGS__)
        const unsigned r8 = lane >> 3, ch8 = ((lane & 7) ^ (((r8 >> 1) & 3) << 1)) << 4;
        {
            const unsigned char* base = (const unsigned char*)wq + (size_t)(b >> 6) * 256 * 8192 + (b & 63) * 128;
            const unsigned v = r8 * 8192 + ch8, d = 8 * 8192 - 1024;
#pragma unroll
            for (int g = 0; g < 4; ++g) { dma4(base + (size_t)g * 32 * 8192, lbase + g * 4096, v, v + d, v + 2 * d, v + 3 * d); ++gi; }
            __builtin_amdgcn_s_barrier();
#pragma unroll
            for (int g = 4; g < 8; ++g) GROUP(W_START, base + (size_t)g * 32 * 8192, lbase + g * 4096, v, v + d, v + 2 * d, v + 3 * d);
        }
        const unsigned vv0 = lg * 256 + ((c ^ (lg << 1)) << 4), vv1 = lg * 256 + ((c ^ ((4 + lg) << 1)) << 4);
        {
            const unsigned char* base = (const unsigned char*)wo + (size_t)(b >> 3) * 128 * 2048 + (b & 7) * 256;
            const unsigned a0 = vv0 + lg * (2048 - 256), a1 = vv1 + lg * (2048 - 256), d = 4 * 2048 - 1024;
#pragma unroll
            for (int g = 0; g < 8; ++g) GROUP(W_START, base + (size_t)g * 16 * 2048, lbase + 2 * SLOT + g * 4096, a0, a1 + d, a0 + 2 * d, a1 + 3 * d);
        }
        {
            const unsigned k0 = lg * 256 + ((c ^ lg) << 4), k1 = lg * 256 + ((c ^ (4 + lg)) << 4), k2 = lg * 256 + ((c ^ (8 + lg)) << 4), k3 = lg * 256 + ((c ^ (12 + lg)) << 4);
            const unsigned char* kp = (const unsigned char*)Kc + (size_t)b * NT * 8192;
            const unsigned char* vp = (const unsigned char*)Vc + (size_t)b * NT * 8192;
            int slot = 4;
#define TILE(W) do { space(4 + t); const unsigned dst = lbase + slot * SLOT; \
                GROUPX(dma4k, W, kp, dst, k0, k1, k2, k3); GROUPX(dma4k, W, kp + 4096, dst + 4096, k0, k1, k2, k3); \
                GROUPX(dma4v, W, vp, dst + 8192, vv0, vv1, vv0, vv1); GROUPX(dma4v, W, vp + 4096, dst + 12288, vv0, vv1, vv0, vv1); \
                kp += 8192; vp += 8192; slot = slot == NSLOT - 1 ? 0 : slot + 1; } while (0)
            int t = 0;
#pragma unroll 1
            for (; t < N_START; ++t) TILE(W_START);
#pragma unroll 1
            for (; t < NT; ++t) TILE(W_MAIN);
        }
        ST(1);
        if (b < NFFN) {
            vmwait<24>(); publish(gi / 2 - 3); vmwait<16>(); publish(gi / 2 - 2);
            const int rb = b & 3, cb = b >> 2;
            int slot = (4 + NT) % NSLOT;
            {
                const size_t o = (size_t)rb * 256 * 7168 + cb * 128;
                const unsigned char* gp = (const unsigned char*)wg + o;
                const unsigned char* up = (const unsigned char*)wu + o;
                const unsigned v = r8 * 7168 + ch8, d = 8 * 7168 - 1024;
#pragma unroll 1
                for (int i = 0; i < 4; ++i) {
                    space(4 + NT + i);
                    const unsigned dst = lbase + slot * SLOT;
                    GROUP(W_TAIL, gp, dst, v, v + d, v + 2 * d, v + 3 * d); GROUP(W_TAIL, gp + 32 * 7168, dst + 4096, v, v + d, v + 2 * d, v + 3 * d);
                    GROUP(W_TAIL, up, dst + 8192, v, v + d, v + 2 * d, v + 3 * d); GROUP(W_TAIL, up + 32 * 7168, dst + 12288, v, v + d, v + 2 * d, v + 3 * d);
                    gp += 64 * 7168; up += 64 * 7168; slot = slot == NSLOT - 1 ? 0 : slot + 1;
                }
            }
            {
                const unsigned char* dp = (const unsigned char*)wd + (size_t)cb * 64 * 2048 + rb * 512;
                const unsigned v = r8 * 2048 + ch8, d = 8 * 2048 - 1024;
#pragma unroll 1
                for (int i = 0; i < 2; ++i) {
                    space(4 + NT + 4 + i);
                    const unsigned dst = lbase + slot * SLOT;
                    GROUP(W_TAIL, dp, dst, v, v + d, v + 2 * d, v + 3 * d); GROUP(W_TAIL, dp + 32 * 2048, dst + 4096, v, v + d, v + 2 * d, v + 3 * d);
                    GROUP(W_TAIL, dp + 128, dst + 8192, v, v + d, v + 2 * d, v + 3 * d); GROUP(W_TAIL, dp + 128 + 32 * 2048, dst + 12288, v, v + d, v + 2 * d, v + 3 * d);
                    dp += 256; slot = slot == NSLOT - 1 ? 0 : slot + 1;
                }
            }
        }
        drain();
        ST(2);
    } else if (wave <= 2) {
        const int hf = wave - 1;
        __builtin_amdgcn_s_barrier();
        unsigned ka[4], va[4];
#pragma unroll
        for (int i = 0; i < 4; ++i) {
            ka[i] = c * 256 + (((4 * i + lg) ^ c) << 4);
            const int row = 4 * lg + (c >> 2);
            va[i] = 8192 + row * 256 + (((4 * hf + i) ^ (row & 7)) << 5) + 8 * (c & 3);
        }
        wait_ge(C_QRDY, 1u);
        v4u qf[4];
#pragma unroll
        for (int i = 0; i < 4; ++i) qf[i] = *(LAS v4u*)(L8 + L_QI + (c & 7) * 272 + (4 * i + lg) * 16);
        if (hf == 0) ST(3);
        float m = -INFINITY, l = 0.f;
        v4f acc[4];
#pragma unroll
        for (int i = 0; i < 4; ++i) acc[i] = (v4f){0, 0, 0, 0};
        const float scale = 0.08838834764831845f;
        unsigned full = 0;
        int slot = 4;
#pragma unroll 1
        for (int t = 0; t < NT; ++t) {
            LAS unsigned char* sp = L8 + slot * SLOT;
            if (full < (unsigned)(9 + 2 * t)) full = wait_ge(C_FULL, 9 + 2 * t);
            CBAR();
            v4u kf[8];
#pragma unroll
            for (int i = 0; i < 4; ++i) { kf[i] = *(LAS v4u*)(sp + ka[i]); kf[4 + i] = *(LAS v4u*)(sp + ka[i] + 4096); }
            v4f s0 = {0, 0, 0, 0}, s1 = {0, 0, 0, 0};
#pragma unroll
            for (int i = 0; i < 4; ++i) { s0 = mfma(kf[i], qf[i], s0); s1 = mfma(kf[4 + i], qf[i], s1); }
            float s[8];
#pragma unroll
            for (int j = 0; j < 4; ++j) { s[j] = rbf(rbf(s0[j]) * scale); s[4 + j] = rbf(rbf(s1[j]) * scale); }
            float mx = fmaxf(fmaxf(fmaxf(s[0], s[1]), fmaxf(s[2], s[3])), fmaxf(fmaxf(s[4], s[5]), fmaxf(s[6], s[7])));
            mx = x32_max(x16_max(mx));
            const float mn = fmaxf(m, mx);
            const float alpha = __builtin_amdgcn_exp2f((m - mn) * LOG2E);
            m = mn;
            float p[8], ps = 0.f;
#pragma unroll
            for (int j = 0; j < 8; ++j) { p[j] = __builtin_amdgcn_exp2f((s[j] - mn) * LOG2E); ps += p[j]; }
            l = l * alpha + ps;
            v4u pf;
#pragma unroll
            for (int j = 0; j < 4; ++j) pf[j] = pack2(p[2 * j], p[2 * j + 1]);
#pragma unroll
            for (int i = 0; i < 4; ++i) acc[i] *= alpha;
            if (full < (unsigned)(10 + 2 * t)) full = wait_ge(C_FULL, 10 + 2 * t);
            CBAR();
            v4u vf[4];
#pragma unroll
            for (int i = 0; i < 4; ++i) vf[i] = trfrag(sp + va[i], 4096);
            asm volatile("s_waitcnt lgkmcnt(0)" : "+v"(vf[0]), "+v"(vf[1]), "+v"(vf[2]), "+v"(vf[3])::"memory");
            lds_st(ctl + (C_DONE0 + hf) * 4, (unsigned)(t + 1));
#pragma unroll
            for (int i = 0; i < 4; ++i) acc[i] = mfma(vf[i], pf, acc[i]);
            slot = slot == NSLOT - 1 ? 0 : slot + 1;
        }
        const unsigned o = WS_ACC + ((unsigned)((b >> 6) * 8 + (c & 7)) * 64 + (b & 63)) * 512 + hf * 256 + (c >> 3) * 64 + lg * 16;
        const bool up = c >= 8;
        st16(o, clamp4(up ? acc[1] : acc[0]));
        st16(o + 128, clamp4(up ? acc[3] : acc[2]));
        if (hf == 0) {
            l = swap16_add(l, l); l = swap32_add(l, l);
            if (lane < 8) st8(WS_ML + b * 64 + lane * 8, (v2u){clampw(m), clampw(l)});
        }
        lds_inc(ctl + C_APUB * 4, lane);
        ST(4 + hf);
    } else if (wave == 3) {
        unsigned vz;
        asm volatile("v_mov_b32 %0, 0" : "=v"(vz));
        const v2u epw = ld8(WS_EPOCH + vz);
        if (lane < 32) *(LAS unsigned*)(L8 + L_CTL + lane * 4) = 0u;
        *(LAS v2u*)(L8 + L_ONE + lane * 8) = (v2u){0x3F803F80u, 0x3F803F80u};
        LGKM0();
        __builtin_amdgcn_s_barrier();
        const v4u sent = {SENT, SENT, SENT, SENT};
#pragma unroll
        for (int i = 0; i < 4; ++i) st16(WS_ACC + ((unsigned)((b >> 6) * 8 + 2 * i + (lane >> 5)) * 64 + (b & 63)) * 512 + (lane & 31) * 16, sent);
        if (lane < 4) st16(WS_ML + b * 64 + lane * 16, sent);
        if (lane < 32) st16(WS_P + (b >> 3) * 4096 + (b & 7) * 512 + lane * 16, sent);
        if (b < NFFN) {
            st16(WS_X + b * 1280 + lane * 16, sent);
            if (lane < 16) st16(WS_X + b * 1280 + 1024 + lane * 16, sent);
            st16(WS_Y + (b >> 2) * 4096 + (b & 3) * 1024 + lane * 16, sent);
        }
        const u64 ep = (u64)epw[0] | ((u64)epw[1] << 32);
        u64 tag = ep + 0x9E3779B97F4A7C15ull;
        tag = (tag ^ (tag >> 30)) * 0xBF58476D1CE4E5B9ull; tag = (tag ^ (tag >> 27)) * 0x94D049BB133111EBull; tag ^= tag >> 31;
        tag += (tag == ep);
        VM0();
        wait_ge(C_QPUB, 4u);
        if (lane == 0) __hip_atomic_store((GAS u64*)(ws + WS_FLAG + b * 128), tag, RLX_AGENT);
        ST(6);
        const int g = b >> 6;
        {
            GAS u64* fp = (GAS u64*)(ws + WS_FLAG) + ((lane >> 4) * 64 + g * 16 + c) * 16;
            #pragma nounroll
            for (;;) { const u64 v = __hip_atomic_load(fp, RLX_AGENT); if (wall(v == tag) || spent()) break; }
        }
        ST(7);
        {
            v4u q[16];
#pragma unroll
            for (int i = 0; i < 16; ++i) q[i] = ld16(WS_QP + (i >> 2) * 16384 + g * 4096 + (i & 3) * 1024 + lane * 16);
#pragma unroll
            for (int i = 0; i < 4; ++i) {
                const v4f s = (__builtin_bit_cast(v4f, q[i]) + __builtin_bit_cast(v4f, q[4 + i])) + (__builtin_bit_cast(v4f, q[8 + i]) + __builtin_bit_cast(v4f, q[12 + i]));
                *(LAS v2u*)(L8 + L_QI + (2 * i + (lane >> 5)) * 272 + (lane & 31) * 8) = (v2u){pack2(s[0], s[1]), pack2(s[2], s[3])};
            }
            LGKM0();
            lds_st(ctl + C_QRDY * 4, 1u);
        }
        {
            GAS u64* fp = (GAS u64*)(ws + WS_FLAG) + lane * 16;
#pragma nounroll
            for (;;) {
                const u64 v0 = __hip_atomic_load(fp, RLX_AGENT), v1 = __hip_atomic_load(fp + 1024, RLX_AGENT), v2 = __hip_atomic_load(fp + 2048, RLX_AGENT), v3 = __hip_atomic_load(fp + 3072, RLX_AGENT);
                if (wall(v0 == tag && v1 == tag && v2 == tag && v3 == tag) || spent()) break;
                __builtin_amdgcn_s_sleep(8);
            }
            lds_st(ctl + C_ALLF * 4, 1u);
            if (b == 0 && lane == 0) __hip_atomic_store((GAS u64*)(ws + WS_EPOCH), tag, RLX_AGENT);
        }
        if (b >= NFFN) {
            const int a = b - NFFN, col = a * 32 + 4 * (lane & 7) + lg;
            const float xv = __uint_as_float((unsigned)x[col] << 16);
            wait_ge(C_OS, 4u);
            v4u pv[4];
#pragma nounroll
            for (;;) {
                CBAR();
                unsigned mx = 0;
#pragma unroll
                for (int j = 0; j < 4; ++j) pv[j] = ld16(WS_P + (8 * j + (lane >> 3)) * 4096 + a * 128 + (lane & 7) * 16);
#pragma unroll
                for (int j = 0; j < 4; ++j) mx = max(mx, umax4(pv[j]));
                if (wall(mx != SENT) || spent()) break;
            }
            float hs;
            {
                v4f e = (__builtin_bit_cast(v4f, pv[0]) + __builtin_bit_cast(v4f, pv[1])) + (__builtin_bit_cast(v4f, pv[2]) + __builtin_bit_cast(v4f, pv[3]));
                hs = rows_reduce4(e); hs += dpp<ROR8>(hs);
            }
            const float h = rbf(xv + rbf(hs));
            ST(14);
            v4u yv[7];
#pragma nounroll
            for (;;) {
                CBAR();
                unsigned mx = 0;
#pragma unroll
                for (int j = 0; j < 7; ++j) yv[j] = ld16(WS_Y + (8 * j + (lane >> 3)) * 4096 + a * 128 + (lane & 7) * 16);
#pragma unroll
                for (int j = 0; j < 7; ++j) mx = max(mx, umax4(yv[j]));
                if (wall(mx != SENT) || spent()) break;
            }
            v4f e = __builtin_bit_cast(v4f, yv[0]);
#pragma unroll
            for (int j = 1; j < 7; ++j) e += __builtin_bit_cast(v4f, yv[j]);
            float ys = rows_reduce4(e); ys += dpp<ROR8>(ys);
            if (c < 8) out[col] = f2bf(h + rbf(ys));
            ST(15);
        }
    } else {
        const int w = wave - 4;
        const int kpos = (lane & 32) | ((lane & 12) << 1) | ((lane & 16) >> 2) | (lane & 3);
        float xres;
        {
            const v4u xa = *(const GAS v4u*)(x + lane * 16), xb = *(const GAS v4u*)(x + lane * 16 + 8);
            const float xm = __uint_as_float((unsigned)x[(b >> 6) * 256 + w * 64 + lane] << 16);
            xres = __uint_as_float((unsigned)x[(b & 3) * 256 + w * 64 + 4 * c + lg] << 16);
            __builtin_amdgcn_s_barrier();
            float v[16];
#pragma unroll
            for (int j = 0; j < 4; ++j) { v[2 * j] = bflo(xa[j]); v[2 * j + 1] = bfhi(xa[j]); v[8 + 2 * j] = bflo(xb[j]); v[9 + 2 * j] = bfhi(xb[j]); }
            float s = 0.f;
#pragma unroll
            for (int j = 0; j < 16; ++j) s += v[j];
            const float mean = wave_sum(s) * (1.0f / 1024);
            float q = 0.f;
#pragma unroll
            for (int j = 0; j < 16; ++j) { const float d = v[j] - mean; q += d * d; }
            const float var = wave_sum(q) * (1.0f / 1024);
            const float sd = rbf(sqrtf(rbf(rbf(var) + 1e-5f)));
            *(LAS unsigned short*)(L8 + L_XS + (w * 64 + kpos) * 2) = f2bf(rbf(xm - rbf(mean)) / sd);
            LGKM0();
            lds_inc(ctl + C_XS * 4, lane);
        }
        const int trow = 4 * lg + (c >> 2);
        const unsigned ta128 = trow * 128 + 8 * (c & 3), tx128 = (trow >> 1) & 3;
        {
            wait_ge(C_XS, 4u);
            v4u xf[8];
#pragma unroll
            for (int k = 0; k < 8; ++k) xf[k] = *(LAS v4u*)(L8 + L_XS + k * 64 + lg * 16);
            wait_ge(C_FULL, 4u);
            v4u wf[8];
#pragma unroll
            for (int k = 0; k < 8; ++k) wf[k] = trfrag(L8 + k * 4096 + ta128 + ((w ^ tx128) << 5), 2048);
            v4f d0 = {0, 0, 0, 0}, d1 = {0, 0, 0, 0};
#pragma unroll
            for (int k = 0; k < 8; k += 2) { d0 = mfma(wf[k], xf[k], d0); d1 = mfma(wf[k + 1], xf[k + 1], d1); }
            d0 += d1;
            if (c == 0) st16(WS_QP + (b >> 6) * 16384 + ((b & 63) * 64 + w * 16 + lg * 4) * 4, __builtin_bit_cast(v4u, d0));
            lds_inc(ctl + C_WQ * 4, lane);
            VM0();
            lds_inc(ctl + C_QPUB * 4, lane);
            if (w == 0) ST(8);
        }
        v4u wof[8];
        {
            wait_ge(C_FULL, 8u);
#pragma unroll
            for (int k = 0; k < 4; ++k)
#pragma unroll
                for (int t = 0; t < 2; ++t) wof[2 * k + t] = trfrag(L8 + 2 * SLOT + k * 8192 + trow * 256 + (((2 * w + t) ^ (trow & 7)) << 5) + 8 * (c & 3), 4096);
            asm volatile("s_waitcnt lgkmcnt(0)" : "+v"(wof[0]), "+v"(wof[1]), "+v"(wof[2]), "+v"(wof[3]), "+v"(wof[4]), "+v"(wof[5]), "+v"(wof[6]), "+v"(wof[7])::"memory");
            lds_inc(ctl + C_WO * 4, lane);
        }
        wait_ge(C_ALLF, 1u);
        wait_ge(C_APUB, 2u);
        {
            const int a = b >> 3;
            v4u av[8]; v2u ml;
#pragma nounroll
            for (;;) {
                CBAR();
                ml = ld8(WS_ML + ((b >> 6) * 64 + lane) * 64 + (a & 7) * 8);
#pragma unroll
                for (int j = 0; j < 8; ++j) av[j] = ld16(WS_ACC + (a * 64 + 8 * (lane >> 3) + j) * 512 + w * 128 + (lane & 7) * 16);
                unsigned mx = max(ml[0], ml[1]);
#pragma unroll
                for (int j = 0; j < 8; ++j) mx = max(mx, umax4(av[j]));
                if (wall(mx != SENT) || spent()) break;
            }
            if (w == 0) ST(9);
            const float ms = __uint_as_float(ml[0]);
            const float M = wave_max(ms);
            const float f = __builtin_amdgcn_exp2f((ms - M) * LOG2E);
            *(LAS float*)(L8 + L_WS + w * 256 + lane * 4) = f;
            CBAR();
            const v4f f0 = *(LAS v4f*)(L8 + L_WS + w * 256 + (lane >> 3) * 32), f1 = *(LAS v4f*)(L8 + L_WS + w * 256 + (lane >> 3) * 32 + 16);
            const float L = wave_sum(f * __uint_as_float(ml[1]));
            v4f e = __builtin_bit_cast(v4f, av[0]) * f0[0];
#pragma unroll
            for (int j = 1; j < 8; ++j) e += __builtin_bit_cast(v4f, av[j]) * (j < 4 ? f0[j & 3] : f1[j & 3]);
            float os = rows_reduce4(e); os += dpp<ROR8>(os);
            const int k = 4 * (lane & 7) + lg;
            if (c < 8) *(LAS unsigned short*)(L8 + L_OS + (w * 32 + (((k & 12) << 1) | ((k & 16) >> 2) | (k & 3))) * 2) = f2bf(os / L);
            LGKM0();
            lds_inc(ctl + C_OS * 4, lane);
            wait_ge(C_OS, 4u);
            v4u of[4];
#pragma unroll
            for (int k2 = 0; k2 < 4; ++k2) of[k2] = *(LAS v4u*)(L8 + L_OS + k2 * 64 + lg * 16);
            v4f d0 = {0, 0, 0, 0}, d1 = {0, 0, 0, 0};
#pragma unroll
            for (int k2 = 0; k2 < 4; ++k2) { d0 = mfma(wof[2 * k2], of[k2], d0); d1 = mfma(wof[2 * k2 + 1], of[k2], d1); }
            if (c < 2) st16(WS_P + a * 4096 + (b & 7) * 512 + w * 128 + c * 64 + lg * 16, clamp4(c ? d1 : d0));
            if (w == 0) ST(10);
        }
        if (b < NFFN) {
            const int rb = b & 3, cb = b >> 2;
            constexpr int F0 = 4 + NT;
            {
                v4u pv[8];
#pragma nounroll
                for (;;) {
                    CBAR();
#pragma unroll
                    for (int j = 0; j < 8; ++j) pv[j] = ld16(WS_P + (8 * (j >> 1) + 2 * lg + (j & 1)) * 4096 + rb * 1024 + w * 256 + c * 16);
                    unsigned mx = 0;
#pragma unroll
                    for (int j = 0; j < 8; ++j) mx = max(mx, umax4(pv[j]));
                    if (wall(mx != SENT) || spent()) break;
                }
                if (w == 0) ST(11);
                const v4f e0 = (__builtin_bit_cast(v4f, pv[0]) + __builtin_bit_cast(v4f, pv[2])) + (__builtin_bit_cast(v4f, pv[4]) + __builtin_bit_cast(v4f, pv[6]));
                const v4f e1 = (__builtin_bit_cast(v4f, pv[1]) + __builtin_bit_cast(v4f, pv[3])) + (__builtin_bit_cast(v4f, pv[5]) + __builtin_bit_cast(v4f, pv[7]));
                const float hs = rows_reduce4(e0) + rows_reduce4(e1);
                const float h = rbf(xres + rbf(hs));
                const int k = 4 * c + lg;
                *(LAS unsigned short*)(L8 + L_HS + (w * 64 + ((k & 32) | ((k & 12) << 1) | ((k & 16) >> 2) | (k & 3))) * 2) = f2bf(h);
                LGKM0();
                lds_inc(ctl + C_HS * 4, lane);
                const float hsum = wave_sum(h);
                const float dd = h - hsum * (1.0f / 64);
                const float hm2 = wave_sum(dd * dd);
                wait_ge(C_FULL, 2 * (F0 + 4));
                v4u gf[8], uf[8];
#pragma unroll
                for (int k2 = 0; k2 < 8; ++k2) {
                    LAS unsigned char* sp = L8 + ((F0 + (k2 >> 1)) % NSLOT) * SLOT + (k2 & 1) * 4096 + ta128 + ((w ^ tx128) << 5);
                    gf[k2] = trfrag(sp, 2048); uf[k2] = trfrag(sp + 8192, 2048);
                }
                wait_ge(C_HS, 4u);
                v4u hf[8];
#pragma unroll
                for (int k2 = 0; k2 < 8; ++k2) hf[k2] = *(LAS v4u*)(L8 + (c < 8 ? L_HS : L_ONE) + k2 * 64 + lg * 16);
                v4f dg = {0, 0, 0, 0}, du = {0, 0, 0, 0};
#pragma unroll
                for (int k2 = 0; k2 < 8; ++k2) { dg = mfma(gf[k2], hf[k2], dg); du = mfma(uf[k2], hf[k2], du); }
                const float gs = (c & 2) ? ((c & 1) ? dg[3] : dg[2]) : ((c & 1) ? dg[1] : dg[0]);
                const float us = (c & 2) ? ((c & 1) ? du[3] : du[2]) : ((c & 1) ? du[1] : du[0]);
                v4f rec = {gs, us, dpp<ROR8>(gs), dpp<ROR8>(us)};
                if (lane == 4) rec = (v4f){hsum, hm2, 0.f, 0.f};
                if (c < 4 || lane == 4) st16(WS_X + b * 1280 + w * 320 + (lane == 4 ? 16 : 4 * lg + c) * 16, clamp4(rec));
                if (w == 0) ST(12);
            }
            {
                wait_ge(C_FULL, 2 * (F0 + 4) + w + 1);
                v4u df[8];
#pragma unroll
                for (int k2 = 0; k2 < 2; ++k2)
#pragma unroll
                    for (int t = 0; t < 4; ++t)
                        df[4 * k2 + t] = trfrag(L8 + ((F0 + 4 + (w >> 1)) % NSLOT) * SLOT + (w & 1) * 8192 + k2 * 4096 + ta128 + ((t ^ tx128) << 5), 2048);
                v4u xv[4]; v2u sv;
#pragma nounroll
                for (;;) {
                    CBAR();
#pragma unroll
                    for (int j = 0; j < 4; ++j) xv[j] = ld16(WS_X + (cb * 4 + j) * 1280 + lg * 320 + c * 16);
                    sv = ld8(WS_X + cb * 5120 + c * 320 + 256);
                    unsigned mx = max(sv[0], sv[1]);
#pragma unroll
                    for (int j = 0; j < 4; ++j) mx = max(mx, umax4(xv[j]));
                    if (wall(mx != SENT) || spent()) break;
                }
                const float ssum = __uint_as_float(sv[0]);
                const float mean = row_sum(ssum) * (1.0f / 1024);
                const float dm = ssum * (1.0f / 64) - mean;
                const float var = row_sum(__uint_as_float(sv[1]) + 64.f * dm * dm) * (1.0f / 1024);
                const float mb = rbf(mean), sd = rbf(sqrtf(rbf(rbf(var) + 1e-5f)));
                const v4f r = (__builtin_bit_cast(v4f, xv[0]) + __builtin_bit_cast(v4f, xv[1])) + (__builtin_bit_cast(v4f, xv[2]) + __builtin_bit_cast(v4f, xv[3]));
                const float gg = rbf((r[0] - mb * r[2]) / sd), uu = rbf((r[1] - mb * r[3]) / sd);
                *(LAS unsigned short*)(L8 + L_AS + w * 128 + kpos * 2) = f2bf(rbf(gelu_tanh(gg)) * uu);
                CBAR();
                const v4u a0 = *(LAS v4u*)(L8 + L_AS + w * 128 + lg * 16), a1 = *(LAS v4u*)(L8 + L_AS + w * 128 + 64 + lg * 16);
                v4f d[4];
#pragma unroll
                for (int t = 0; t < 4; ++t) { d[t] = mfma(df[t], a0, (v4f){0, 0, 0, 0}); d[t] = mfma(df[4 + t], a1, d[t]); }
                const v4f y = (c & 2) ? ((c & 1) ? d[3] : d[2]) : ((c & 1) ? d[1] : d[0]);
                if (c < 4) st16(WS_Y + cb * 4096 + rb * 1024 + w * 256 + c * 64 + lg * 16, clamp4(y));
                if (w == 0) ST(13);
            }
        }
    }
}

extern "C" void kernel_launch(void* const* d_in, const int* in_sizes, int n_in, void* d_out, int out_size, void* d_ws, size_t ws_size, hipStream_t stream) {
    (void)in_sizes; (void)n_in; (void)out_size; (void)ws_size;
    (void)hipFuncSetAttribute((const void*)fused, hipFuncAttributeMaxDynamicSharedMemorySize, L_END);
    fused<<<NWG, 512, L_END, stream>>>((const unsigned short*)d_in[0], (const unsigned short*)d_in[1], (const unsigned short*)d_in[2], (const unsigned short*)d_in[3],
                                       (const unsigned short*)d_in[4], (const unsigned short*)d_in[6], (const unsigned short*)d_in[7], (const unsigned short*)d_in[8],
                                       (unsigned short*)d_out, (unsigned char*)d_ws);
}
```

```cpp
#define STAMP 1
#define PIN 0
#include <hip/hip_runtime.h>
#include <stdint.h>

typedef unsigned v4u __attribute__((ext_vector_type(4)));
typedef unsigned v2u __attribute__((ext_vector_type(2)));
typedef float v4f __attribute__((ext_vector_type(4)));
typedef __bf16 v8bf __attribute__((ext_vector_type(8)));
typedef short v4s __attribute__((ext_vector_type(4)));
typedef unsigned long long u64;
#define GAS __attribute__((address_space(1)))
#define LAS __attribute__((address_space(3)))
#define RLX_AGENT __ATOMIC_RELAXED, __HIP_MEMORY_SCOPE_AGENT

#define W_START 20
#define W_MAIN 32
#define W_TAIL 16
#define N_START 4

constexpr int NWG = 256, NFFN = 224, NT = 32;
constexpr int NSLOT = 9, SLOT = 16384;
constexpr unsigned WS_EPOCH = 0, WS_FLAG = 4096, WS_QP = WS_FLAG + 256 * 128, WS_ACC = WS_QP + 4 * 4096 * 4, WS_ML = WS_ACC + 32 * 64 * 512,
                   WS_P = WS_ML + 256 * 64, WS_X = WS_P + 32 * 4096, WS_Y = WS_X + 56 * 4 * 1280, WS_END = WS_Y + 56 * 4096, WS_STAMP = 4u << 20;
constexpr int L_QI = NSLOT * SLOT, L_XS = L_QI + 8 * 272, L_OS = L_XS + 512, L_HS = L_OS + 256, L_ONE = L_HS + 512, L_WS = L_ONE + 512, L_AS = L_WS + 1024,
              L_CTL = L_AS + 512, L_STAMP = L_CTL + 128, L_END = L_STAMP + 512;
enum { C_FULL = 0, C_DONE0, C_DONE1, C_WQ, C_WO, C_XS, C_QPUB, C_QRDY, C_ALLF, C_APUB, C_OS, C_HS };
static_assert(L_END <= 160 * 1024 && (L_XS & 15) == 0, "lds");
constexpr unsigned SENT = 0xFFFFFFFFu;
constexpr unsigned SPIN = 1u << 22;

__device__ __forceinline__ float bflo(unsigned u) { return __uint_as_float(u << 16); }
__device__ __forceinline__ float bfhi(unsigned u) { return __uint_as_float(u & 0xffff0000u); }
__device__ __forceinline__ unsigned short f2bf(float f) { __bf16 b = (__bf16)f; return __builtin_bit_cast(unsigned short, b); }
__device__ __forceinline__ float rbf(float f) { return __uint_as_float((unsigned)f2bf(f) << 16); }
__device__ __forceinline__ unsigned pack2(float a, float b) { return (unsigned)f2bf(a) | ((unsigned)f2bf(b) << 16); }
__device__ __forceinline__ unsigned clampw(float f) { const unsigned u = __float_as_uint(f); return u < 0xFFFFFFFEu ? u : 0xFFFFFFFEu; }
__device__ __forceinline__ v4u clamp4(v4f v) { return (v4u){clampw(v[0]), clampw(v[1]), clampw(v[2]), clampw(v[3])}; }
__device__ __forceinline__ unsigned umax4(v4u v) { return max(max(v[0], v[1]), max(v[2], v[3])); }

template <int CTRL> __device__ __forceinline__ float dpp(float v) {
    return __builtin_bit_cast(float, __builtin_amdgcn_update_dpp(0, __builtin_bit_cast(int, v), CTRL, 0xf, 0xf, true));
}
constexpr int ROR1 = 0x121, ROR2 = 0x122, ROR4 = 0x124, ROR8 = 0x128;
__device__ __forceinline__ float row_sum(float v) { v += dpp<ROR8>(v); v += dpp<ROR4>(v); v += dpp<ROR2>(v); v += dpp<ROR1>(v); return v; }
__device__ __forceinline__ float row_max(float v) { v = fmaxf(v, dpp<ROR8>(v)); v = fmaxf(v, dpp<ROR4>(v)); v = fmaxf(v, dpp<ROR2>(v)); v = fmaxf(v, dpp<ROR1>(v)); return v; }
__device__ __forceinline__ float swap16_add(float a, float b) {
    const auto r = __builtin_amdgcn_permlane16_swap(__float_as_uint(a), __float_as_uint(b), false, false);
    return __uint_as_float(r[0]) + __uint_as_float(r[1]);
}
__device__ __forceinline__ float swap32_add(float a, float b) {
    const auto r = __builtin_amdgcn_permlane32_swap(__float_as_uint(a), __float_as_uint(b), false, false);
    return __uint_as_float(r[0]) + __uint_as_float(r[1]);
}
__device__ __forceinline__ float x16_max(float a) {
    const auto r = __builtin_amdgcn_permlane16_swap(__float_as_uint(a), __float_as_uint(a), false, false);
    return fmaxf(__uint_as_float(r[0]), __uint_as_float(r[1]));
}
__device__ __forceinline__ float x32_max(float a) {
    const auto r = __builtin_amdgcn_permlane32_swap(__float_as_uint(a), __float_as_uint(a), false, false);
    return fmaxf(__uint_as_float(r[0]), __uint_as_float(r[1]));
}
__device__ __forceinline__ float wave_sum(float v) { v = row_sum(v); v = swap16_add(v, v); return swap32_add(v, v); }
__device__ __forceinline__ float wave_max(float v) { return x32_max(x16_max(row_max(v))); }
__device__ __forceinline__ float rows_reduce4(v4f v) { return swap32_add(swap16_add(v[0], v[1]), swap16_add(v[2], v[3])); }

__device__ __forceinline__ v4f mfma(v4u a, v4u b, v4f c) {
    return __builtin_amdgcn_mfma_f32_16x16x32_bf16(__builtin_bit_cast(v8bf, a), __builtin_bit_cast(v8bf, b), c, 0, 0, 0);
}
__device__ __forceinline__ v2u trread(LAS unsigned char* p) {
    return __builtin_bit_cast(v2u, __builtin_amdgcn_ds_read_tr16_b64_v4i16((LAS v4s*)p));
}
__device__ __forceinline__ v4u trfrag(LAS unsigned char* p, int hi_off) { const v2u a = trread(p), b = trread(p + hi_off); return (v4u){a[0], a[1], b[0], b[1]}; }

#define CBAR() asm volatile("" ::: "memory")
#define LGKM0() asm volatile("s_waitcnt lgkmcnt(0)" ::: "memory")
#define VM0() asm volatile("s_waitcnt vmcnt(0)" ::: "memory")
template <int N> __device__ __forceinline__ void vmwait() { asm volatile("s_waitcnt vmcnt(%0)" ::"n"(N) : "memory"); }

#define POL_W " nt"
#define POL_K " nt"
#define POL_V " nt"
#define DMA4(NAME, POL) \
__device__ __forceinline__ void NAME(const void* sbase, unsigned dst, unsigned v0, unsigned v1, unsigned v2, unsigned v3) { \
    unsigned keep; \
    asm volatile("s_mov_b32 %0, m0\n\ts_mov_b32 m0, %2\n\ts_nop 0\n\t" \
                 "global_load_lds_dwordx4 %3, %1" POL "\n\tglobal_load_lds_dwordx4 %4, %1 offset:1024" POL "\n\t" \
                 "global_load_lds_dwordx4 %5, %1 offset:2048" POL "\n\tglobal_load_lds_dwordx4 %6, %1 offset:3072" POL "\n\ts_mov_b32 m0, %0" \
                 : "=&s"(keep) : "s"(sbase), "s"(dst), "v"(v0), "v"(v1), "v"(v2), "v"(v3) : "memory"); \
}
DMA4(dma4, POL_W)
DMA4(dma4k, POL_K)
DMA4(dma4v, POL_V)
__device__ __forceinline__ unsigned lds_ld(unsigned addr) {
    unsigned v;
    asm volatile("ds_read_b32 %0, %1\n\ts_waitcnt lgkmcnt(0)" : "=&v"(v) : "v"(addr) : "memory");
    return __builtin_amdgcn_readfirstlane(v);
}
__device__ __forceinline__ void lds_st(unsigned addr, unsigned v) { asm volatile("ds_write_b32 %0, %1" ::"v"(addr), "v"(v) : "memory"); }
__device__ __forceinline__ void lds_inc(unsigned addr, int lane) { if (lane == 0) asm volatile("ds_add_u32 %0, %1" ::"v"(addr), "v"(1u) : "memory"); }

__device__ __forceinline__ bool wall(bool p) { return __builtin_amdgcn_ballot_w64(!p) == 0ull; }
__device__ __forceinline__ float gelu_tanh(float x) {
    const float inner = 0.7978845608028654f * (x + 0.044715f * x * x * x);
    return 0.5f * x * (1.0f + tanhf(inner));
}

#define ST(i) do { if (lane == 0) *(volatile LAS u64*)(L8 + L_STAMP + (i) * 8) = __builtin_amdgcn_s_memrealtime(); } while (0)

__global__ void __launch_bounds__(512, 2) fused(const unsigned short* __restrict__ x, const unsigned short* __restrict__ Kc, const unsigned short* __restrict__ Vc,
                                                const unsigned short* __restrict__ wq, const unsigned short* __restrict__ wo, const unsigned short* __restrict__ wg,
                                                const unsigned short* __restrict__ wu, const unsigned short* __restrict__ wd, unsigned short* __restrict__ out,
                                                unsigned char* __restrict__ ws) {
    extern __shared__ __attribute__((aligned(16))) unsigned char smem[];
    LAS unsigned char* L8 = (LAS unsigned char*)smem;
    const unsigned lbase = (unsigned)(uintptr_t)L8;
    const int lane = threadIdx.x & 63, wave = __builtin_amdgcn_readfirstlane(threadIdx.x >> 6), b = blockIdx.x;
    const int c = lane & 15, lg = lane >> 4;
    const unsigned ctl = lbase + L_CTL;
    const __amdgpu_buffer_rsrc_t rs = __builtin_amdgcn_make_buffer_rsrc(ws, 0, WS_END, 0x00020000);
    const float LOG2E = 1.4426950408889634f;
    const u64 cyc0 = __builtin_amdgcn_s_memtime();
    unsigned bud = SPIN;
    auto wait_ge = [&](int word, unsigned need) -> unsigned {
        unsigned v, t;
        bud = __builtin_amdgcn_readfirstlane(bud); need = __builtin_amdgcn_readfirstlane(need);
        asm volatile(".Lw%=:\n\tds_read_b32 %1, %3\n\ts_waitcnt lgkmcnt(0)\n\tv_readfirstlane_b32 %0, %1\n\ts_cmp_ge_u32 %0, %4\n\ts_cbranch_scc1 .Ld%=\n\t"
                     "s_sleep 1\n\ts_sub_u32 %2, %2, 1\n\ts_cbranch_scc0 .Lw%=\n\ts_mov_b32 %2, 0\n.Ld%=:"
                     : "=&s"(v), "=&v"(t), "+s"(bud) : "v"(ctl + word * 4), "s"(need) : "memory", "scc");
        return v;
    };
    auto spent = [&]() -> bool { const bool d = bud < 8u; bud = d ? 0u : bud - 8u; return d; };
    auto ld16 = [&](unsigned off) -> v4u { return __builtin_amdgcn_raw_buffer_load_b128(rs, off, 0, 16); };
    auto ld8 = [&](unsigned off) -> v2u { return __builtin_amdgcn_raw_buffer_load_b64(rs, off, 0, 16); };
    auto st16 = [&](unsigned off, v4u v) { __builtin_amdgcn_raw_buffer_store_b128(v, rs, off, 0, 16); };
    auto st8 = [&](unsigned off, v2u v) { __builtin_amdgcn_raw_buffer_store_b64(v, rs, off, 0, 16); };

    if (wave == 0) {
        ST(0);
        int gi = 0;
        int pub = 0;
        auto publish = [&](int units) { if (units > pub) { pub = units; lds_st(ctl + C_FULL * 4, (unsigned)units); } };
        auto drain = [&]() {
            vmwait<24>(); publish(gi / 2 - 3); vmwait<16>(); publish(gi / 2 - 2); vmwait<8>(); publish(gi / 2 - 1); vmwait<0>(); publish(gi / 2);
        };
        unsigned seen = 0;
        auto space = [&](int f) {
            if (f < NSLOT) return;
            const int p = f - NSLOT;
            if (p < 4) {
                const int wd_ = p < 2 ? C_WQ : C_WO;
                if (lds_ld(ctl + wd_ * 4) < 4u) { drain(); wait_ge(wd_, 4u); }
            } else if (seen < (unsigned)(p - 3)) {
                seen = min(lds_ld(ctl + C_DONE0 * 4), lds_ld(ctl + C_DONE1 * 4));
                if (seen < (unsigned)(p - 3)) { drain(); seen = min(wait_ge(C_DONE0, p - 3), wait_ge(C_DONE1, p - 3)); }
            }
        };
#define GROUPX(FN, W, SB, DST, V0, V1, V2, V3) do { vmwait<(W) - 4>(); publish((4 * gi - (W) + 4) >> 3); FN(SB, DST, V0, V1, V2, V3); ++gi; } while (0)
#define GROUP(...) GROUPX(dma4, __VA_ARGS__)
        const unsigned r8 = lane >> 3, ch8 = ((lane & 7) ^ (((r8 >> 1) & 3) << 1)) << 4;
        {
            const unsigned char* base = (const unsigned char*)wq + (size_t)(b >> 6) * 256 * 8192 + (b & 63) * 128;
            const unsigned v = r8 * 8192 + ch8, d = 8 * 8192 - 1024;
#pragma unroll
            for (int g = 0; g < 4; ++g) { dma4(base + (size_t)g * 32 * 8192, lbase + g * 4096, v, v + d, v + 2 * d, v + 3 * d); ++gi; }
            __builtin_amdgcn_s_barrier();
#pragma unroll
            for (int g = 4; g < 8; ++g) GROUP(W_START, base + (size_t)g * 32 * 8192, lbase + g * 4096, v, v + d, v + 2 * d, v + 3 * d);
        }
        const unsigned vv0 = lg * 256 + ((c ^ (lg << 1)) << 4), vv1 = lg * 256 + ((c ^ ((4 + lg) << 1)) << 4);
        {
            const unsigned char* base = (const unsigned char*)wo + (size_t)(b >> 3) * 128 * 2048 + (b & 7) * 256;
            const unsigned a0 = vv0 + lg * (2048 - 256), a1 = vv1 + lg * (2048 - 256), d = 4 * 2048 - 1024;
#pragma unroll
            for (int g = 0; g < 8; ++g) GROUP(W_START, base + (size_t)g * 16 * 2048, lbase + 2 * SLOT + g * 4096, a0, a1 + d, a0 + 2 * d, a1 + 3 * d);
        }
        {
            const unsigned k0 = lg * 256 + ((c ^ lg) << 4), k1 = lg * 256 + ((c ^ (4 + lg)) << 4), k2 = lg * 256 + ((c ^ (8 + lg)) << 4), k3 = lg * 256 + ((c ^ (12 + lg)) << 4);
            const unsigned char* kp = (const unsigned char*)Kc + (size_t)b * NT * 8192;
            const unsigned char* vp = (const unsigned char*)Vc + (size_t)b * NT * 8192;
            int slot = 4;
#define TILE(W) do { space(4 + t); const unsigned dst = lbase + slot * SLOT; \
                GROUPX(dma4k, W, kp, dst, k0, k1, k2, k3); GROUPX(dma4k, W, kp + 4096, dst + 4096, k0, k1, k2, k3); \
                GROUPX(dma4v, W, vp, dst + 8192, vv0, vv1, vv0, vv1); GROUPX(dma4v, W, vp + 4096, dst + 12288, vv0, vv1, vv0, vv1); \
                kp += 8192; vp += 8192; slot = slot == NSLOT - 1 ? 0 : slot + 1; } while (0)
            int t = 0;
#pragma unroll 1
            for (; t < N_START; ++t) TILE(W_START);
#pragma unroll 1
            for (; t < NT; ++t) TILE(W_MAIN);
        }
        ST(1);
        if (b < NFFN) {
            vmwait<24>(); publish(gi / 2 - 3); vmwait<16>(); publish(gi / 2 - 2);
            const int rb = b & 3, cb = b >> 2;
            int slot = (4 + NT) % NSLOT;
            {
                const size_t o = (size_t)rb * 256 * 7168 + cb * 128;
                const unsigned char* gp = (const unsigned char*)wg + o;
                const unsigned char* up = (const unsigned char*)wu + o;
                const unsigned v = r8 * 7168 + ch8, d = 8 * 7168 - 1024;
#pragma unroll 1
                for (int i = 0; i < 4; ++i) {
                    space(4 + NT + i);
                    const unsigned dst = lbase + slot * SLOT;
                    GROUP(W_TAIL, gp, dst, v, v + d, v + 2 * d, v + 3 * d); GROUP(W_TAIL, gp + 32 * 7168, dst + 4096, v, v + d, v + 2 * d, v + 3 * d);
                    GROUP(W_TAIL, up, dst + 8192, v, v + d, v + 2 * d, v + 3 * d); GROUP(W_TAIL, up + 32 * 7168, dst + 12288, v, v + d, v + 2 * d, v + 3 * d);
                    gp += 64 * 7168; up += 64 * 7168; slot = slot == NSLOT - 1 ? 0 : slot + 1;
                }
            }
            {
                const unsigned char* dp = (const unsigned char*)wd + (size_t)cb * 64 * 2048 + rb * 512;
                const unsigned v = r8 * 2048 + ch8, d = 8 * 2048 - 1024;
#pragma unroll 1
                for (int i = 0; i < 2; ++i) {
                    space(4 + NT + 4 + i);
                    const unsigned dst = lbase + slot * SLOT;
                    GROUP(W_TAIL, dp, dst, v, v + d, v + 2 * d, v + 3 * d); GROUP(W_TAIL, dp + 32 * 2048, dst + 4096, v, v + d, v + 2 * d, v + 3 * d);
                    GROUP(W_TAIL, dp + 128, dst + 8192, v, v + d, v + 2 * d, v + 3 * d); GROUP(W_TAIL, dp + 128 + 32 * 2048, dst + 12288, v, v + d, v + 2 * d, v + 3 * d);
                    dp += 256; slot = slot == NSLOT - 1 ? 0 : slot + 1;
                }
            }
        }
        drain();
        ST(2);
    } else if (wave <= 2) {
        const int hf = wave - 1;
        __builtin_amdgcn_s_barrier();
        unsigned ka[4], va[4];
#pragma unroll
        for (int i = 0; i < 4; ++i) {
            ka[i] = c * 256 + (((4 * i + lg) ^ c) << 4);
            const int row = 4 * lg + (c >> 2);
            va[i] = 8192 + row * 256 + (((4 * hf + i) ^ (row & 7)) << 5) + 8 * (c & 3);
        }
        wait_ge(C_QRDY, 1u);
        v4u qf[4];
#pragma unroll
        for (int i = 0; i < 4; ++i) qf[i] = *(LAS v4u*)(L8 + L_QI + (c & 7) * 272 + (4 * i + lg) * 16);
        if (hf == 0) ST(3);
        float m = -INFINITY, l = 0.f;
        v4f acc[4];
#pragma unroll
        for (int i = 0; i < 4; ++i) acc[i] = (v4f){0, 0, 0, 0};
        const float scale = 0.08838834764831845f;
        unsigned full = 0;
        int slot = 4;
#pragma unroll 1
        for (int t = 0; t < NT; ++t) {
            LAS unsigned char* sp = L8 + slot * SLOT;
            if (full < (unsigned)(9 + 2 * t)) full = wait_ge(C_FULL, 9 + 2 * t);
            CBAR();
            v4u kf[8];
#pragma unroll
            for (int i = 0; i < 4; ++i) { kf[i] = *(LAS v4u*)(sp + ka[i]); kf[4 + i] = *(LAS v4u*)(sp + ka[i] + 4096); }
            v4f s0 = {0, 0, 0, 0}, s1 = {0, 0, 0, 0};
#pragma unroll
            for (int i = 0; i < 4; ++i) { s0 = mfma(kf[i], qf[i], s0); s1 = mfma(kf[4 + i], qf[i], s1); }
            float s[8];
#pragma unroll
            for (int j = 0; j < 4; ++j) { s[j] = rbf(rbf(s0[j]) * scale); s[4 + j] = rbf(rbf(s1[j]) * scale); }
            float mx = fmaxf(fmaxf(fmaxf(s[0], s[1]), fmaxf(s[2], s[3])), fmaxf(fmaxf(s[4], s[5]), fmaxf(s[6], s[7])));
            mx = x32_max(x16_max(mx));
            const float mn = fmaxf(m, mx);
            const float alpha = __builtin_amdgcn_exp2f((m - mn) * LOG2E);
            m = mn;
            float p[8], ps = 0.f;
#pragma unroll
            for (int j = 0; j < 8; ++j) { p[j] = __builtin_amdgcn_exp2f((s[j] - mn) * LOG2E); ps += p[j]; }
            l = l * alpha + ps;
            v4u pf;
#pragma unroll
            for (int j = 0; j < 4; ++j) pf[j] = pack2(p[2 * j], p[2 * j + 1]);
#pragma unroll
            for (int i = 0; i < 4; ++i) acc[i] *= alpha;
            if (full < (unsigned)(10 + 2 * t)) full = wait_ge(C_FULL, 10 + 2 * t);
            CBAR();
            v4u vf[4];
#pragma unroll
            for (int i = 0; i < 4; ++i) vf[i] = trfrag(sp + va[i], 4096);
            asm volatile("s_waitcnt lgkmcnt(0)" : "+v"(vf[0]), "+v"(vf[1]), "+v"(vf[2]), "+v"(vf[3])::"memory");
            lds_st(ctl + (C_DONE0 + hf) * 4, (unsigned)(t + 1));
#pragma unroll
            for (int i = 0; i < 4; ++i) acc[i] = mfma(vf[i], pf, acc[i]);
            slot = slot == NSLOT - 1 ? 0 : slot + 1;
        }
        const unsigned o = WS_ACC + ((unsigned)((b >> 6) * 8 + (c & 7)) * 64 + (b & 63)) * 512 + hf * 256 + (c >> 3) * 64 + lg * 16;
        const bool up = c >= 8;
        st16(o, clamp4(up ? acc[1] : acc[0]));
        st16(o + 128, clamp4(up ? acc[3] : acc[2]));
        if (hf == 0) {
            l = swap16_add(l, l); l = swap32_add(l, l);
            if (lane < 8) st8(WS_ML + b * 64 + lane * 8, (v2u){clampw(m), clampw(l)});
        }
        lds_inc(ctl + C_APUB * 4, lane);
        ST(4 + hf);
    } else if (wave == 3) {
        unsigned vz;
        asm volatile("v_mov_b32 %0, 0" : "=v"(vz));
        const v2u epw = ld8(WS_EPOCH + vz);
        if (lane < 32) *(LAS unsigned*)(L8 + L_CTL + lane * 4) = 0u;
        *(LAS v2u*)(L8 + L_ONE + lane * 8) = (v2u){0x3F803F80u, 0x3F803F80u};
        LGKM0();
        __builtin_amdgcn_s_barrier();
        const v4u sent = {SENT, SENT, SENT, SENT};
#pragma unroll
        for (int i = 0; i < 4; ++i) st16(WS_ACC + ((unsigned)((b >> 6) * 8 + 2 * i + (lane >> 5)) * 64 + (b & 63)) * 512 + (lane & 31) * 16, sent);
        if (lane < 4) st16(WS_ML + b * 64 + lane * 16, sent);
        if (lane < 32) st16(WS_P + (b >> 3) * 4096 + (b & 7) * 512 + lane * 16, sent);
        if (b < NFFN) {
            st16(WS_X + b * 1280 + lane * 16, sent);
            if (lane < 16) st16(WS_X + b * 1280 + 1024 + lane * 16, sent);
            st16(WS_Y + (b >> 2) * 4096 + (b & 3) * 1024 + lane * 16, sent);
        }
        const u64 ep = (u64)epw[0] | ((u64)epw[1] << 32);
        u64 tag = ep + 0x9E3779B97F4A7C15ull;
        tag = (tag ^ (tag >> 30)) * 0xBF58476D1CE4E5B9ull; tag = (tag ^ (tag >> 27)) * 0x94D049BB133111EBull; tag ^= tag >> 31;
        tag += (tag == ep);
        VM0();
        wait_ge(C_QPUB, 4u);
        if (lane == 0) __hip_atomic_store((GAS u64*)(ws + WS_FLAG + b * 128), tag, RLX_AGENT);
        ST(6);
        const int g = b >> 6;
        {
            GAS u64* fp = (GAS u64*)(ws + WS_FLAG) + ((lane >> 4) * 64 + g * 16 + c) * 16;
            #pragma nounroll
            for (;;) { const u64 v = __hip_atomic_load(fp, RLX_AGENT); if (wall(v == tag) || spent()) break; }
        }
        ST(7);
        {
            v4u q[16];
#pragma unroll
            for (int i = 0; i < 16; ++i) q[i] = ld16(WS_QP + (i >> 2) * 16384 + g * 4096 + (i & 3) * 1024 + lane * 16);
#pragma unroll
            for (int i = 0; i < 4; ++i) {
                const v4f s = (__builtin_bit_cast(v4f, q[i]) + __builtin_bit_cast(v4f, q[4 + i])) + (__builtin_bit_cast(v4f, q[8 + i]) + __builtin_bit_cast(v4f, q[12 + i]));
                *(LAS v2u*)(L8 + L_QI + (2 * i + (lane >> 5)) * 272 + (lane & 31) * 8) = (v2u){pack2(s[0], s[1]), pack2(s[2], s[3])};
            }
            LGKM0();
            lds_st(ctl + C_QRDY * 4, 1u);
        }
        {
            GAS u64* fp = (GAS u64*)(ws + WS_FLAG) + lane * 16;
#pragma nounroll
            for (;;) {
                const u64 v0 = __hip_atomic_load(fp, RLX_AGENT), v1 = __hip_atomic_load(fp + 1024, RLX_AGENT), v2 = __hip_atomic_load(fp + 2048, RLX_AGENT), v3 = __hip_atomic_load(fp + 3072, RLX_AGENT);
                if (wall(v0 == tag && v1 == tag && v2 == tag && v3 == tag) || spent()) break;
                __builtin_amdgcn_s_sleep(8);
            }
            lds_st(ctl + C_ALLF * 4, 1u);
            if (b == 0 && lane == 0) __hip_atomic_store((GAS u64*)(ws + WS_EPOCH), tag, RLX_AGENT);
        }
        if (b >= NFFN) {
            const int a = b - NFFN, col = a * 32 + 4 * (lane & 7) + lg;
            const float xv = __uint_as_float((unsigned)x[col] << 16);
            wait_ge(C_OS, 4u);
            v4u pv[4];
#pragma nounroll
            for (;;) {
                CBAR();
                unsigned mx = 0;
#pragma unroll
                for (int j = 0; j < 4; ++j) pv[j] = ld16(WS_P + (8 * j + (lane >> 3)) * 4096 + a * 128 + (lane & 7) * 16);
#pragma unroll
                for (int j = 0; j < 4; ++j) mx = max(mx, umax4(pv[j]));
                if (wall(mx != SENT) || spent()) break;
            }
            float hs;
            {
                v4f e = (__builtin_bit_cast(v4f, pv[0]) + __builtin_bit_cast(v4f, pv[1])) + (__builtin_bit_cast(v4f, pv[2]) + __builtin_bit_cast(v4f, pv[3]));
                hs = rows_reduce4(e); hs += dpp<ROR8>(hs);
            }
            const float h = rbf(xv + rbf(hs));
            ST(14);
            v4u yv[7];
#pragma nounroll
            for (;;) {
                CBAR();
                unsigned mx = 0;
#pragma unroll
                for (int j = 0; j < 7; ++j) yv[j] = ld16(WS_Y + (8 * j + (lane >> 3)) * 4096 + a * 128 + (lane & 7) * 16);
#pragma unroll
                for (int j = 0; j < 7; ++j) mx = max(mx, umax4(yv[j]));
                if (wall(mx != SENT) || spent()) break;
            }
            v4f e = __builtin_bit_cast(v4f, yv[0]);
#pragma unroll
            for (int j = 1; j < 7; ++j) e += __builtin_bit_cast(v4f, yv[j]);
            float ys = rows_reduce4(e); ys += dpp<ROR8>(ys);
            if (c < 8) out[col] = f2bf(h + rbf(ys));
            ST(15);
        }
    } else {
        const int w = wave - 4;
        const int kpos = (lane & 32) | ((lane & 12) << 1) | ((lane & 16) >> 2) | (lane & 3);
        float xres;
        {
            const v4u xa = *(const GAS v4u*)(x + lane * 16), xb = *(const GAS v4u*)(x + lane * 16 + 8);
            const float xm = __uint_as_float((unsigned)x[(b >> 6) * 256 + w * 64 + lane] << 16);
            xres = __uint_as_float((unsigned)x[(b & 3) * 256 + w * 64 + 4 * c + lg] << 16);
            __builtin_amdgcn_s_barrier();
            float v[16];
#pragma unroll
            for (int j = 0; j < 4; ++j) { v[2 * j] = bflo(xa[j]); v[2 * j + 1] = bfhi(xa[j]); v[8 + 2 * j] = bflo(xb[j]); v[9 + 2 * j] = bfhi(xb[j]); }
            float s = 0.f;
#pragma unroll
            for (int j = 0; j < 16; ++j) s += v[j];
            const float mean = wave_sum(s) * (1.0f / 1024);
            float q = 0.f;
#pragma unroll
            for (int j = 0; j < 16; ++j) { const float d = v[j] - mean; q += d * d; }
            const float var = wave_sum(q) * (1.0f / 1024);
            const float sd = rbf(sqrtf(rbf(rbf(var) + 1e-5f)));
            *(LAS unsigned short*)(L8 + L_XS + (w * 64 + kpos) * 2) = f2bf(rbf(xm - rbf(mean)) / sd);
            LGKM0();
            lds_inc(ctl + C_XS * 4, lane);
        }
        const int trow = 4 * lg + (c >> 2);
        const unsigned ta128 = trow * 128 + 8 * (c & 3), tx128 = (trow >> 1) & 3;
        {
            wait_ge(C_XS, 4u);
            v4u xf[8];
#pragma unroll
            for (int k = 0; k < 8; ++k) xf[k] = *(LAS v4u*)(L8 + L_XS + k * 64 + lg * 16);
            wait_ge(C_FULL, 4u);
            v4u wf[8];
#pragma unroll
            for (int k = 0; k < 8; ++k) wf[k] = trfrag(L8 + k * 4096 + ta128 + ((w ^ tx128) << 5), 2048);
            v4f d0 = {0, 0, 0, 0}, d1 = {0, 0, 0, 0};
#pragma unroll
            for (int k = 0; k < 8; k += 2) { d0 = mfma(wf[k], xf[k], d0); d1 = mfma(wf[k + 1], xf[k + 1], d1); }
            d0 += d1;
            if (c == 0) st16(WS_QP + (b >> 6) * 16384 + ((b & 63) * 64 + w * 16 + lg * 4) * 4, __builtin_bit_cast(v4u, d0));
            lds_inc(ctl + C_WQ * 4, lane);
            VM0();
            lds_inc(ctl + C_QPUB * 4, lane);
            if (w == 0) ST(8);
        }
        v4u wof[8];
        {
            wait_ge(C_FULL, 8u);
#pragma unroll
            for (int k = 0; k < 4; ++k)
#pragma unroll
                for (int t = 0; t < 2; ++t) wof[2 * k + t] = trfrag(L8 + 2 * SLOT + k * 8192 + trow * 256 + (((2 * w + t) ^ (trow & 7)) << 5) + 8 * (c & 3), 4096);
            asm volatile("s_waitcnt lgkmcnt(0)" : "+v"(wof[0]), "+v"(wof[1]), "+v"(wof[2]), "+v"(wof[3]), "+v"(wof[4]), "+v"(wof[5]), "+v"(wof[6]), "+v"(wof[7])::"memory");
            lds_inc(ctl + C_WO * 4, lane);
        }
        wait_ge(C_ALLF, 1u);
        wait_ge(C_APUB, 2u);
        {
            const int a = b >> 3;
            v4u av[8]; v2u ml;
#pragma nounroll
            for (;;) {
                CBAR();
                ml = ld8(WS_ML + ((b >> 6) * 64 + lane) * 64 + (a & 7) * 8);
#pragma unroll
                for (int j = 0; j < 8; ++j) av[j] = ld16(WS_ACC + (a * 64 + 8 * (lane >> 3) + j) * 512 + w * 128 + (lane & 7) * 16);
                unsigned mx = max(ml[0], ml[1]);
#pragma unroll
                for (int j = 0; j < 8; ++j) mx = max(mx, umax4(av[j]));
                if (wall(mx != SENT) || spent()) break;
            }
            if (w == 0) ST(9);
            const float ms = __uint_as_float(ml[0]);
            const float M = wave_max(ms);
            const float f = __builtin_amdgcn_exp2f((ms - M) * LOG2E);
            *(LAS float*)(L8 + L_WS + w * 256 + lane * 4) = f;
            CBAR();
            const v4f f0 = *(LAS v4f*)(L8 + L_WS + w * 256 + (lane >> 3) * 32), f1 = *(LAS v4f*)(L8 + L_WS + w * 256 + (lane >> 3) * 32 + 16);
            const float L = wave_sum(f * __uint_as_float(ml[1]));
            v4f e = __builtin_bit_cast(v4f, av[0]) * f0[0];
#pragma unroll
            for (int j = 1; j < 8; ++j) e += __builtin_bit_cast(v4f, av[j]) * (j < 4 ? f0[j & 3] : f1[j & 3]);
            float os = rows_reduce4(e); os += dpp<ROR8>(os);
            const int k = 4 * (lane & 7) + lg;
            if (c < 8) *(LAS unsigned short*)(L8 + L_OS + (w * 32 + (((k & 12) << 1) | ((k & 16) >> 2) | (k & 3))) * 2) = f2bf(os / L);
            LGKM0();
            lds_inc(ctl + C_OS * 4, lane);
            wait_ge(C_OS, 4u);
            v4u of[4];
#pragma unroll
            for (int k2 = 0; k2 < 4; ++k2) of[k2] = *(LAS v4u*)(L8 + L_OS + k2 * 64 + lg * 16);
            v4f d0 = {0, 0, 0, 0}, d1 = {0, 0, 0, 0};
#pragma unroll
            for (int k2 = 0; k2 < 4; ++k2) { d0 = mfma(wof[2 * k2], of[k2], d0); d1 = mfma(wof[2 * k2 + 1], of[k2], d1); }
            if (c < 2) st16(WS_P + a * 4096 + (b & 7) * 512 + w * 128 + c * 64 + lg * 16, clamp4(c ? d1 : d0));
            if (w == 0) ST(10);
        }
        if (b < NFFN) {
            const int rb = b & 3, cb = b >> 2;
            constexpr int F0 = 4 + NT;
            {
                v4u pv[8];
#pragma nounroll
                for (;;) {
                    CBAR();
#pragma unroll
                    for (int j = 0; j < 8; ++j) pv[j] = ld16(WS_P + (8 * (j >> 1) + 2 * lg + (j & 1)) * 4096 + rb * 1024 + w * 256 + c * 16);
                    unsigned mx = 0;
#pragma unroll
                    for (int j = 0; j < 8; ++j) mx = max(mx, umax4(pv[j]));
                    if (wall(mx != SENT) || spent()) break;
                }
                if (w == 0) ST(11);
                const v4f e0 = (__builtin_bit_cast(v4f, pv[0]) + __builtin_bit_cast(v4f, pv[2])) + (__builtin_bit_cast(v4f, pv[4]) + __builtin_bit_cast(v4f, pv[6]));
                const v4f e1 = (__builtin_bit_cast(v4f, pv[1]) + __builtin_bit_cast(v4f, pv[3])) + (__builtin_bit_cast(v4f, pv[5]) + __builtin_bit_cast(v4f, pv[7]));
                const float hs = rows_reduce4(e0) + rows_reduce4(e1);
                const float h = rbf(xres + rbf(hs));
                const int k = 4 * c + lg;
                *(LAS unsigned short*)(L8 + L_HS + (w * 64 + ((k & 32) | ((k & 12) << 1) | ((k & 16) >> 2) | (k & 3))) * 2) = f2bf(h);
                LGKM0();
                lds_inc(ctl + C_HS * 4, lane);
                const float hsum = wave_sum(h);
                const float dd = h - hsum * (1.0f / 64);
                const float hm2 = wave_sum(dd * dd);
                wait_ge(C_FULL, 2 * (F0 + 4));
                v4u gf[8], uf[8];
#pragma unroll
                for (int k2 = 0; k2 < 8; ++k2) {
                    LAS unsigned char* sp = L8 + ((F0 + (k2 >> 1)) % NSLOT) * SLOT + (k2 & 1) * 4096 + ta128 + ((w ^ tx128) << 5);
                    gf[k2] = trfrag(sp, 2048); uf[k2] = trfrag(sp + 8192, 2048);
                }
                wait_ge(C_HS, 4u);
                v4u hf[8];
#pragma unroll
                for (int k2 = 0; k2 < 8; ++k2) hf[k2] = *(LAS v4u*)(L8 + (c < 8 ? L_HS : L_ONE) + k2 * 64 + lg * 16);
                v4f dg = {0, 0, 0, 0}, du = {0, 0, 0, 0};
#pragma unroll
                for (int k2 = 0; k2 < 8; ++k2) { dg = mfma(gf[k2], hf[k2], dg); du = mfma(uf[k2], hf[k2], du); }
                const float gs = (c & 2) ? ((c & 1) ? dg[3] : dg[2]) : ((c & 1) ? dg[1] : dg[0]);
                const float us = (c & 2) ? ((c & 1) ? du[3] : du[2]) : ((c & 1) ? du[1] : du[0]);
                v4f rec = {gs, us, dpp<ROR8>(gs), dpp<ROR8>(us)};
                if (lane == 4) rec = (v4f){hsum, hm2, 0.f, 0.f};
                if (c < 4 || lane == 4) st16(WS_X + b * 1280 + w * 320 + (lane == 4 ? 16 : 4 * lg + c) * 16, clamp4(rec));
                if (w == 0) ST(12);
            }
            {
                wait_ge(C_FULL, 2 * (F0 + 4) + w + 1);
                v4u df[8];
#pragma unroll
                for (int k2 = 0; k2 < 2; ++k2)
#pragma unroll
                    for (int t = 0; t < 4; ++t)
                        df[4 * k2 + t] = trfrag(L8 + ((F0 + 4 + (w >> 1)) % NSLOT) * SLOT + (w & 1) * 8192 + k2 * 4096 + ta128 + ((t ^ tx128) << 5), 2048);
                v4u xv[4]; v2u sv;
#pragma nounroll
                for (;;) {
                    CBAR();
#pragma unroll
                    for (int j = 0; j < 4; ++j) xv[j] = ld16(WS_X + (cb * 4 + j) * 1280 + lg * 320 + c * 16);
                    sv = ld8(WS_X + cb * 5120 + c * 320 + 256);
                    unsigned mx = max(sv[0], sv[1]);
#pragma unroll
                    for (int j = 0; j < 4; ++j) mx = max(mx, umax4(xv[j]));
                    if (wall(mx != SENT) || spent()) break;
                }
                const float ssum = __uint_as_float(sv[0]);
                const float mean = row_sum(ssum) * (1.0f / 1024);
                const float dm = ssum * (1.0f / 64) - mean;
                const float var = row_sum(__uint_as_float(sv[1]) + 64.f * dm * dm) * (1.0f / 1024);
                const float mb = rbf(mean), sd = rbf(sqrtf(rbf(rbf(var) + 1e-5f)));
                const v4f r = (__builtin_bit_cast(v4f, xv[0]) + __builtin_bit_cast(v4f, xv[1])) + (__builtin_bit_cast(v4f, xv[2]) + __builtin_bit_cast(v4f, xv[3]));
                const float gg = rbf((r[0] - mb * r[2]) / sd), uu = rbf((r[1] - mb * r[3]) / sd);
                *(LAS unsigned short*)(L8 + L_AS + w * 128 + kpos * 2) = f2bf(rbf(gelu_tanh(gg)) * uu);
                CBAR();
                const v4u a0 = *(LAS v4u*)(L8 + L_AS + w * 128 + lg * 16), a1 = *(LAS v4u*)(L8 + L_AS + w * 128 + 64 + lg * 16);
                v4f d[4];
#pragma unroll
                for (int t = 0; t < 4; ++t) { d[t] = mfma(df[t], a0, (v4f){0, 0, 0, 0}); d[t] = mfma(df[4 + t], a1, d[t]); }
                const v4f y = (c & 2) ? ((c & 1) ? d[3] : d[2]) : ((c & 1) ? d[1] : d[0]);
                if (c < 4) st16(WS_Y + cb * 4096 + rb * 1024 + w * 256 + c * 64 + lg * 16, clamp4(y));
                if (w == 0) ST(13);
            }
        }
    }
    LGKM0();
    __builtin_amdgcn_s_barrier();
    if (wave == 0 && b >= NFFN) {
        const u64 t0 = *(volatile LAS u64*)(L8 + L_STAMP), tp = *(volatile LAS u64*)(L8 + L_STAMP + (PIN & 15) * 8);
        u64 add = tp - t0;
        if (PIN == 16) add = (__builtin_amdgcn_s_memtime() - cyc0) / 24;
        if (add > 20000) add = 20000;
        const u64 until = t0 + 6000 + add;
        for (int i = 0; i < 1000000 && __builtin_amdgcn_s_memrealtime() < until; ++i) __builtin_amdgcn_s_sleep(1);
    }
}

extern "C" void kernel_launch(void* const* d_in, const int* in_sizes, int n_in, void* d_out, int out_size, void* d_ws, size_t ws_size, hipStream_t stream) {
    (void)in_sizes; (void)n_in; (void)out_size; (void)ws_size;
    (void)hipFuncSetAttribute((const void*)fused, hipFuncAttributeMaxDynamicSharedMemorySize, L_END);
    fused<<<NWG, 512, L_END, stream>>>((const unsigned short*)d_in[0], (const unsigned short*)d_in[1], (const unsigned short*)d_in[2], (const unsigned short*)d_in[3],
                                       (const unsigned short*)d_in[4], (const unsigned short*)d_in[6], (const unsigned short*)d_in[7], (const unsigned short*)d_in[8],
                                       (unsigned short*)d_out, (unsigned char*)d_ws);
}
```

```cpp
#define DRY 0
#include <hip/hip_runtime.h>
#include <stdint.h>

typedef unsigned v4u __attribute__((ext_vector_type(4)));
typedef unsigned v2u __attribute__((ext_vector_type(2)));
typedef float v4f __attribute__((ext_vector_type(4)));
typedef float v2f __attribute__((ext_vector_type(2)));
typedef __bf16 v8bf __attribute__((ext_vector_type(8)));
typedef __bf16 v2bf __attribute__((ext_vector_type(2)));
typedef short v4s __attribute__((ext_vector_type(4)));
typedef unsigned long long u64;
#define GAS __attribute__((address_space(1)))
#define LAS __attribute__((address_space(3)))
#define RLX_AGENT __ATOMIC_RELAXED, __HIP_MEMORY_SCOPE_AGENT

#define W_MAIN 28
#define W_GU 24
#define W_DN 32
#define KP 3
#define W_HEAD 32
#define HEADG 8
#define DLY_F 100
#define NOSTOP 0
#define PREMAX 24
#define XPOLL 11
#define GATE_DN 1
#define DLY_Q 28
#define DLY_ML 16
#define DLY_1 8
#define DLY_2 4
#define DLY_3 12
#define DLY_4 50
#define PACE_A 330
#define PACE_B 443
#define PACE_K 20
#define QEXP 8
#define CHAIN 2
#define W_MIN 20
#define W_MAX 36

constexpr int NWG = 256, NT = 32;
constexpr int NSLOT = 9, SLOT = 16384, FD = 4, FSTR = 1088, NP = NT / 2;
constexpr int G0 = 8, GKV = G0 + 4 * NT;
constexpr unsigned XREC = 1152;
constexpr unsigned WS_EPOCH = 0, WS_FLAG = 4096, WS_QP = WS_FLAG + 256 * 8, WS_ACC = WS_QP + 4 * 4096 * 8, WS_ML = WS_ACC + 32 * 64 * 256,
                   WS_P = WS_ML + 256 * 128, WS_X = WS_P + 32 * 4096, WS_Y = WS_X + 224 * XREC, WS_END = WS_Y + 56 * 4096, WS_STAMP = 4u << 20;
constexpr int L_QI = NSLOT * SLOT, L_XS = L_QI + 8 * 272, L_PART = L_XS + 512, L_HS = L_PART + 16 * 272, L_ONE = L_HS + 512, L_W = L_ONE + 512, L_AS = L_W + 256,
              L_PF = L_AS + 512, L_CTL = L_PF + FD * FSTR, L_STAMP = L_CTL + 128, L_END = L_STAMP + 512;
enum { C_KDONE = 0, C_DONE0, C_DONE1, C_STOP, C_FULL, C_PRDY, C_WQ, C_WO, C_XS, C_WQL, C_QRDY, C_OK, C_WRDY, C_H1, C_H2 };
constexpr unsigned sched() { unsigned m = 0; for (int p = 0; p < NP - 1 - KP; ++p) m |= 2u << (2 * p); for (int s = 2 * (NP - 1) - KP; s < 2 * (NP - 1); ++s) m |= 1u << s; return m; }
static_assert(W_MIN >= (CHAIN == 3 ? 24 : CHAIN == 2 ? 20 : 16) && W_MAX <= 36 && (CHAIN || !PACE_K) && SLOT == 0x4000, "window");
static_assert(L_END <= 160 * 1024 && (L_XS & 15) == 0 && (L_CTL & 15) == 0 && KP < FD && (4 + NT) % NSLOT == 0, "lds");
constexpr unsigned SENT = 0xFFFFFFFFu;
constexpr int SPIN = 1 << 22;

__device__ __forceinline__ float bflo(unsigned u) { return __uint_as_float(u << 16); }
__device__ __forceinline__ float bfhi(unsigned u) { return __uint_as_float(u & 0xffff0000u); }
__device__ __forceinline__ unsigned short f2bf(float f) { __bf16 b = (__bf16)f; return __builtin_bit_cast(unsigned short, b); }
__device__ __forceinline__ unsigned pack2(float a, float b) { return __builtin_bit_cast(unsigned, __builtin_convertvector((v2f){a, b}, v2bf)); }
__device__ __forceinline__ float rbf(float f) { return __uint_as_float(pack2(0.f, f)); }
__device__ __forceinline__ unsigned clampu(unsigned u) { return u < 0xFFFFFFFEu ? u : 0xFFFFFFFEu; }
__device__ __forceinline__ unsigned clampw(float f) { return clampu(__float_as_uint(f)); }
__device__ __forceinline__ unsigned umax4(v4u v) { return max(max(v[0], v[1]), max(v[2], v[3])); }
__device__ __forceinline__ v4f asf(v4u v) { return __builtin_bit_cast(v4f, v); }

template <int CTRL> __device__ __forceinline__ float dpp(float v) {
    return __builtin_bit_cast(float, __builtin_amdgcn_update_dpp(0, __builtin_bit_cast(int, v), CTRL, 0xf, 0xf, true));
}
constexpr int ROR1 = 0x121, ROR2 = 0x122, ROR4 = 0x124, ROR8 = 0x128;
__device__ __forceinline__ float row_sum(float v) { v += dpp<ROR8>(v); v += dpp<ROR4>(v); v += dpp<ROR2>(v); v += dpp<ROR1>(v); return v; }
__device__ __forceinline__ float row_max(float v) { v = fmaxf(v, dpp<ROR8>(v)); v = fmaxf(v, dpp<ROR4>(v)); v = fmaxf(v, dpp<ROR2>(v)); v = fmaxf(v, dpp<ROR1>(v)); return v; }
__device__ __forceinline__ float swap16_add(float a, float b) {
    const auto r = __builtin_amdgcn_permlane16_swap(__float_as_uint(a), __float_as_uint(b), false, false);
    return __uint_as_float(r[0]) + __uint_as_float(r[1]);
}
__device__ __forceinline__ float swap32_add(float a, float b) {
    const auto r = __builtin_amdgcn_permlane32_swap(__float_as_uint(a), __float_as_uint(b), false, false);
    return __uint_as_float(r[0]) + __uint_as_float(r[1]);
}
__device__ __forceinline__ float x16_max(float a) {
    const auto r = __builtin_amdgcn_permlane16_swap(__float_as_uint(a), __float_as_uint(a), false, false);
    return fmaxf(__uint_as_float(r[0]), __uint_as_float(r[1]));
}
__device__ __forceinline__ float x32_max(float a) {
    const auto r = __builtin_amdgcn_permlane32_swap(__float_as_uint(a), __float_as_uint(a), false, false);
    return fmaxf(__uint_as_float(r[0]), __uint_as_float(r[1]));
}
__device__ __forceinline__ float wave_sum(float v) { v = row_sum(v); v = swap16_add(v, v); return swap32_add(v, v); }
__device__ __forceinline__ float wave_max(float v) { return x32_max(x16_max(row_max(v))); }
__device__ __forceinline__ float rows_reduce4(v4f v) { return swap32_add(swap16_add(v[0], v[1]), swap16_add(v[2], v[3])); }

__device__ __forceinline__ v4f mfma(v4u a, v4u b, v4f c) {
    return __builtin_amdgcn_mfma_f32_16x16x32_bf16(__builtin_bit_cast(v8bf, a), __builtin_bit_cast(v8bf, b), c, 0, 0, 0);
}
__device__ __forceinline__ v2u trread(LAS unsigned char* p) {
    return __builtin_bit_cast(v2u, __builtin_amdgcn_ds_read_tr16_b64_v4i16((LAS v4s*)p));
}
__device__ __forceinline__ v4u trfrag(LAS unsigned char* p, int hi_off) { const v2u a = trread(p), b = trread(p + hi_off); return (v4u){a[0], a[1], b[0], b[1]}; }

#define CBAR() asm volatile("" ::: "memory")
#define LGKM0() asm volatile("s_waitcnt lgkmcnt(0)" ::: "memory")
#define VM0() asm volatile("s_waitcnt vmcnt(0)" ::: "memory")
#define BAR() asm volatile("s_barrier" ::: "memory")
#define SB() __builtin_amdgcn_sched_barrier(0)
#define PINV(x) asm volatile("" : "+v"(x))
template <int N> __device__ __forceinline__ void vmwait() { asm volatile("s_waitcnt vmcnt(%0)" ::"n"(N) : "memory"); }
template <int N> __device__ __forceinline__ void nap() { if (N > 0) asm volatile("s_sleep %0" ::"n"(N) : "memory"); }

__device__ __forceinline__ void dma4(const void* sbase, unsigned dst, unsigned v0, unsigned v1, unsigned v2, unsigned v3) {
    unsigned keep;
    asm volatile("s_mov_b32 %0, m0\n\ts_mov_b32 m0, %2\n\ts_nop 0\n\t"
                 "global_load_lds_dwordx4 %3, %1 nt\n\tglobal_load_lds_dwordx4 %4, %1 offset:1024 nt\n\t"
                 "global_load_lds_dwordx4 %5, %1 offset:2048 nt\n\tglobal_load_lds_dwordx4 %6, %1 offset:3072 nt\n\ts_mov_b32 m0, %0"
                 : "=&s"(keep) : "s"(sbase), "s"(dst), "v"(v0), "v"(v1), "v"(v2), "v"(v3) : "memory");
}
__device__ __forceinline__ int lds_ld(unsigned addr) {
    unsigned v;
    asm volatile("ds_read_b32 %0, %1\n\ts_waitcnt lgkmcnt(0)" : "=&v"(v) : "v"(addr) : "memory");
    return __builtin_amdgcn_readfirstlane(v);
}
__device__ __forceinline__ v4u lds_ld4(unsigned addr) {
    v4u v;
    asm volatile("ds_read_b128 %0, %1\n\ts_waitcnt lgkmcnt(0)" : "=&v"(v) : "v"(addr) : "memory");
    return v;
}
__device__ __forceinline__ void lds_st(unsigned addr, int v) { asm volatile("ds_write_b32 %0, %1" ::"v"(addr), "v"(v) : "memory"); }
__device__ __forceinline__ void lds_inc(unsigned addr, int n = 1) { asm volatile("s_mov_b64 exec, 1\n\tds_add_u32 %0, %1\n\ts_mov_b64 exec, -1" ::"v"(addr), "v"(n) : "memory"); }
__device__ __forceinline__ float max3(float a, float b, float c) { float d; asm("v_max3_f32 %0, %1, %2, %3" : "=v"(d) : "v"(a), "v"(b), "v"(c)); return d; }
__device__ __forceinline__ float max_over8(float a) {
    float t;
    asm("s_nop 1\n\tv_max_f32_dpp %0, %0, %0 row_ror:8 row_mask:0xf bank_mask:0xf\n\tv_mov_b32 %1, %0\n\ts_nop 1\n\tv_permlane16_swap_b32 %0, %1\n\ts_nop 0\n\tv_max_f32 %0, %0, %1\n\t"
        "v_mov_b32 %1, %0\n\ts_nop 1\n\tv_permlane32_swap_b32 %0, %1\n\ts_nop 0\n\tv_max_f32 %0, %0, %1" : "+v"(a), "=&v"(t));
    return a;
}
__device__ __forceinline__ int rfl(unsigned v) { return __builtin_amdgcn_readfirstlane(v); }

__device__ __forceinline__ bool wnone(bool p) { return __builtin_amdgcn_ballot_w64(p) == 0ull; }

#define ST(i) do {} while (0)
#define STV(i, v) do {} while (0)
#define DUMP(mask) do {} while (0)

__global__ void __launch_bounds__(512, 2) fused(const unsigned short* __restrict__ x, const unsigned short* __restrict__ wq, unsigned char* __restrict__ ws,
                                                const unsigned short* __restrict__ Kc, const unsigned short* __restrict__ Vc, const unsigned short* __restrict__ wo,
                                                const unsigned short* __restrict__ wg, const unsigned short* __restrict__ wu, const unsigned short* __restrict__ wd,
                                                unsigned short* __restrict__ out) {
    const u64 t_entry = __builtin_amdgcn_s_memrealtime();
    extern __shared__ __attribute__((aligned(16))) unsigned char smem[];
    LAS unsigned char* L8 = (LAS unsigned char*)smem;
    const unsigned lbase = (unsigned)(uintptr_t)L8;
    const int lane = threadIdx.x & 63, wave = __builtin_amdgcn_readfirstlane(threadIdx.x >> 6), b = blockIdx.x;
    const int c = lane & 15, lg = lane >> 4;
    const int xc = b & 7, jj = b >> 3, r = xc * 32 + jj;
    const int g = r >> 6, head = r >> 3, ocb = r & 7;
    const int qr = (r >> 4) & 3, qc = g * 16 + (r & 15);
    const bool ffn = jj < 28;
    const int rb = xc >> 1, cb = (xc & 1) * 28 + jj;
    const unsigned ctl = lbase + L_CTL;
    const unsigned r8 = lane >> 3, ch8 = ((lane & 7) ^ (((r8 >> 1) & 3) << 1)) << 4;
    const int fa = xc * 4 + jj - 28, fcol = fa * 32 + 4 * (lane & 7) + lg;
    v4u xa, xb; v2u epw; unsigned xm, xr;
    {
        const unsigned v = (qr * 256 + (wave & 3) * 64 + r8) * 8192 + qc * 128 + ch8, d = 8 * 8192 - 1024, u = v + 32 * 8192;
        unsigned keep;
        asm volatile("s_cmp_gt_i32 %[wv], 3\n\ts_cbranch_scc1 .Lskip%=\n\t"
                     "global_load_dwordx4 %[xa], %[o0], %[x]\n\tglobal_load_dwordx4 %[xb], %[o0], %[x] offset:16\n\t"
                     "global_load_ushort %[xm], %[o1], %[x]\n\tglobal_load_ushort %[xr], %[o2], %[x]\n\tglobal_load_dwordx2 %[ep], %[z], %[ws] sc1\n\t"
                     "s_mov_b32 %[keep], m0\n\ts_mov_b32 m0, %[d0]\n\ts_nop 0\n\t"
                     "global_load_lds_dwordx4 %[v0], %[b0] nt\n\tglobal_load_lds_dwordx4 %[v1], %[b0] offset:1024 nt\n\t"
                     "global_load_lds_dwordx4 %[v2], %[b0] offset:2048 nt\n\tglobal_load_lds_dwordx4 %[v3], %[b0] offset:3072 nt\n\t"
                     "s_mov_b32 m0, %[d1]\n\ts_nop 0\n\t"
                     "global_load_lds_dwordx4 %[u0], %[b0] nt\n\tglobal_load_lds_dwordx4 %[u1], %[b0] offset:1024 nt\n\t"
                     "global_load_lds_dwordx4 %[u2], %[b0] offset:2048 nt\n\tglobal_load_lds_dwordx4 %[u3], %[b0] offset:3072 nt\n\t"
                     "s_mov_b32 m0, %[keep]\n.Lskip%=:"
                     : [xa] "=&v"(xa), [xb] "=&v"(xb), [xm] "=&v"(xm), [xr] "=&v"(xr), [ep] "=&v"(epw), [keep] "=&s"(keep)
                     : [wv] "s"(wave), [x] "s"(x), [ws] "s"(ws), [z] "v"(WS_EPOCH), [o0] "v"(lane * 32), [o1] "v"((qr * 256 + (wave & 3) * 64 + lane) * 2),
                       [o2] "v"((ffn ? rb * 256 + (wave & 3) * 64 + 4 * c + lg : fcol) * 2), [b0] "s"(wq), [d0] "s"(lbase + wave * 8192), [d1] "s"(lbase + wave * 8192 + 4096),
                       [v0] "v"(v), [v1] "v"(v + d), [v2] "v"(v + 2 * d), [v3] "v"(v + 3 * d), [u0] "v"(u), [u1] "v"(u + d), [u2] "v"(u + 2 * d), [u3] "v"(u + 3 * d)
                     : "memory", "scc");
    }
    const unsigned vv0 = lg * 256 + ((c ^ (lg << 1)) << 4), vv1 = lg * 256 + ((c ^ ((4 + lg) << 1)) << 4);
    const unsigned k0 = lg * 256 + ((c ^ lg) << 4), k1 = k0 ^ 64, k2 = k0 ^ 128, k3 = k0 ^ 192;
    const unsigned char* kp = (const unsigned char*)Kc + (size_t)r * NT * 8192;
    const unsigned char* vp = (const unsigned char*)Vc + (size_t)r * NT * 8192;
    const unsigned char* op = (const unsigned char*)wo + (size_t)head * 128 * 2048 + ocb * 256;
    const unsigned o0 = vv0 + lg * (2048 - 256), o1 = vv1 + lg * (2048 - 256) + 7168, o2 = o0 + 2 * 7168, o3 = o1 + 2 * 7168;
    {
#define G4(B, A0, A1, A2, A3) "s_nop 0\n\tglobal_load_lds_dwordx4 %[" #A0 "], %[" #B "] nt\n\tglobal_load_lds_dwordx4 %[" #A1 "], %[" #B "] offset:1024 nt\n\t" \
                              "global_load_lds_dwordx4 %[" #A2 "], %[" #B "] offset:2048 nt\n\tglobal_load_lds_dwordx4 %[" #A3 "], %[" #B "] offset:3072 nt\n\t"
        unsigned keep;
        asm volatile("s_cmp_lg_i32 %[wv], 4\n\ts_cbranch_scc1 .Lskiq%=\n\ts_mov_b32 %[keep], m0\n\t"
                     "s_add_i32 m0, %[lb], 0x10000\n\t" G4(kp, k0, k1, k2, k3) "s_add_i32 m0, %[lb], 0x12000\n\t" G4(vp, v0, v1, v0, v1)
                     "s_add_i32 m0, %[lb], 0x8000\n\t" G4(op, o0, o1, o2, o3)
                     "s_add_i32 m0, %[lb], 0x11000\n\t" G4(kq, k0, k1, k2, k3) "s_add_i32 m0, %[lb], 0x13000\n\t" G4(vq, v0, v1, v0, v1)
                     "s_add_i32 m0, %[lb], 0x9000\n\t" G4(p1, o0, o1, o2, o3)
                     "s_add_i32 m0, %[lb], 0xa000\n\t" G4(p2, o0, o1, o2, o3) "s_add_i32 m0, %[lb], 0xb000\n\t" G4(p3, o0, o1, o2, o3)
                     "s_mov_b32 m0, %[keep]\n.Lskiq%=:"
                     : [keep] "=&s"(keep)
                     : [wv] "s"(wave), [lb] "s"(lbase), [kp] "s"(kp), [vp] "s"(vp), [op] "s"(op), [kq] "s"(kp + 4096), [vq] "s"(vp + 4096), [p1] "s"(op + 32768), [p2] "s"(op + 65536),
                       [p3] "s"(op + 98304), [k0] "v"(k0), [k1] "v"(k1), [k2] "v"(k2), [k3] "v"(k3), [v0] "v"(vv0), [v1] "v"(vv1), [o0] "v"(o0), [o1] "v"(o1), [o2] "v"(o2), [o3] "v"(o3)
                     : "memory", "scc");
    }
    asm volatile("" ::"s"(wg), "s"(wu));
    __amdgpu_buffer_rsrc_t rs = __builtin_amdgcn_make_buffer_rsrc(ws, 0, WS_END, 0x00020000);
    const float LOG2E = 1.4426950408889634f;
    int bud = SPIN;
    auto wait_ge = [&](int word, int need) -> int {
        int v; unsigned t;
        bud = __builtin_amdgcn_readfirstlane(bud); need = __builtin_amdgcn_readfirstlane(need);
        asm volatile(".Lw%=:\n\tds_read_b32 %1, %3\n\ts_waitcnt lgkmcnt(0)\n\tv_readfirstlane_b32 %0, %1\n\ts_cmp_ge_i32 %0, %4\n\ts_cbranch_scc1 .Ld%=\n\t"
                     "s_sub_i32 %2, %2, 1\n\ts_cmp_ge_i32 %2, 0\n\ts_cbranch_scc1 .Lw%=\n.Ld%=:"
                     : "=&s"(v), "=&v"(t), "+s"(bud) : "v"(ctl + word * 4), "s"(need) : "memory", "scc");
        return v;
    };
    auto spent = [&]() -> bool { bud -= 8; return bud < 0; };
    auto ld16 = [&](unsigned off) -> v4u { return __builtin_amdgcn_raw_buffer_load_b128(rs, off, 0, 16); };
    auto ld8 = [&](unsigned off) -> v2u { return __builtin_amdgcn_raw_buffer_load_b64(rs, off, 0, 16); };
    auto st16 = [&](unsigned off, v4u v) { __builtin_amdgcn_raw_buffer_store_b128(v, rs, off, 0, 16); };
    auto st8 = [&](unsigned off, v2u v) { __builtin_amdgcn_raw_buffer_store_b64(v, rs, off, 0, 16); };
    auto st4 = [&](unsigned off, unsigned v) { __builtin_amdgcn_raw_buffer_store_b32(v, rs, off, 0, 16); };
#define REPOLL(N, V, OFF) do { u64 again = dry; _Pragma("nounroll") for (;;) { bool nd[N]; \
                _Pragma("unroll") for (int j = 0; j < N; ++j) { nd[j] = umax4(V[j]) == SENT; again |= __builtin_amdgcn_ballot_w64(nd[j]); } \
                if (again == 0) break; \
                if (spent()) break; \
                again = 0; CBAR(); \
                _Pragma("unroll") for (int j = 0; j < N; ++j) if (nd[j]) V[j] = ld16(OFF); } } while (0)

    if (wave == 4) {
        ST(38);
        int gi = 0;
        int pub = 0;
        auto publish = [&](int v) { pub = max(pub, v); lds_st(ctl + C_FULL * 4, pub); };
        auto drain = [&]() {
            vmwait<36>(); publish(gi - 9); vmwait<32>(); publish(gi - 8); vmwait<28>(); publish(gi - 7); vmwait<24>(); publish(gi - 6); vmwait<20>(); publish(gi - 5); vmwait<16>(); publish(gi - 4);
            vmwait<12>(); publish(gi - 3); vmwait<8>(); publish(gi - 2); vmwait<4>(); publish(gi - 1); vmwait<0>(); publish(gi);
        };
        int kseen = 0, vseen = 0;
        auto refresh = [&]() -> int { const v4u cw = lds_ld4(ctl); kseen = rfl(cw[0]); vseen = min(rfl(cw[1]), rfl(cw[2])); return rfl(cw[3]); };
        int nst = 1, lst = 0;
        auto needK = [&](int n) { if (kseen < n) { refresh(); if (kseen < n) { ++nst; lst = n; drain(); kseen = wait_ge(C_KDONE, n); } } };
        auto needV = [&](int n) { if (vseen < n) { refresh(); if (vseen < n) { ++nst; lst = n; drain(); vseen = min(wait_ge(C_DONE0, n), wait_ge(C_DONE1, n)); } } };
#define GROUP(W, SB, DST, V0, V1, V2, V3) do { vmwait<(W) - 4>(); publish((4 * gi - (W) + 4) >> 2); dma4(SB, DST, V0, V1, V2, V3); ++gi; } while (0)
        gi = HEADG;
        BAR();
#pragma unroll 1
        for (int i = HEADG - 4; i < 8; ++i) GROUP(W_HEAD, op + (size_t)i * 16 * 2048, lbase + 2 * SLOT + i * 4096, o0, o1, o2, o3);
        ST(33);
        kp += 8192; vp += 8192;
        GROUP(W_HEAD, kp, lbase + 5 * SLOT, k0, k1, k2, k3); GROUP(W_HEAD, kp + 4096, lbase + 5 * SLOT + 4096, k0, k1, k2, k3);
        GROUP(W_HEAD, vp, lbase + 5 * SLOT + 8192, vv0, vv1, vv0, vv1); GROUP(W_HEAD, vp + 4096, lbase + 5 * SLOT + 12288, vv0, vv1, vv0, vv1);
        kp += 8192; vp += 8192;
        {
            int ks = 6, vs = 6, tk = 2, tv = 2;
            unsigned sc = sched();
            int tref = PACE_A * 16 + 2 * PACE_B;
            unsigned now = (unsigned)__builtin_amdgcn_s_memrealtime();
            unsigned ko[16], vo[8];
#pragma unroll
            for (int i = 0; i < 4; ++i) { ko[4 * i] = k0 + 4096 * i; ko[4 * i + 1] = k1 + 4096 * i; ko[4 * i + 2] = k2 + 4096 * i; ko[4 * i + 3] = k3 + 4096 * i; vo[2 * i] = vv0 + 4096 * i; vo[2 * i + 1] = vv1 + 4096 * i; }
#define LINK(N, L) "s_cmp_ge_i32 %[nw], " #N "\n\ts_waitcnt vmcnt(" #N ")\n\ts_cbranch_scc1 ." #L "%=\n\t"
#define STR2(x) #x
#define STR(x) STR2(x)
#define WAITV(L) LINK(32, L) LINK(28, L) LINK(24, L) LINK(20, L) "s_waitcnt vmcnt(16)\n." #L "%=:\n\t"
#define G4V(L, A0, A1, A2, A3) WAITV(L) "global_load_lds_dwordx4 %[" #A0 "], %[b] nt\n\tglobal_load_lds_dwordx4 %[" #A1 "], %[b] offset:1024 nt\n\t" \
            "global_load_lds_dwordx4 %[" #A2 "], %[b] offset:2048 nt\n\tglobal_load_lds_dwordx4 %[" #A3 "], %[b] offset:3072 nt\n\tds_write_b32 %[pa], %[pv]\n\tv_add_u32 %[pv], 1, %[pv]\n\t"
#define BLOCK(B, D0, D1, O0, O1, O2, O3, O4, O5, O6, O7, O8, O9, O10, O11, O12, O13, O14, O15) do { unsigned keep, pv = gi - ((nw + 3) >> 2); u64 tm; \
            asm volatile("s_mov_b32 %[keep], m0\n\ts_mov_b32 m0, %[d0]\n\t" G4V(La, a0, a1, a2, a3) "s_add_i32 m0, %[d0], 0x1000\n\t" G4V(Lb, a4, a5, a6, a7) \
                         "s_memrealtime %[now]\n\ts_mov_b32 m0, %[d1]\n\t" G4V(Lc, a8, a9, a10, a11) "s_add_i32 m0, %[d1], 0x1000\n\t" WAITV(Ld) "s_waitcnt lgkmcnt(0)\n\t" \
                         "global_load_lds_dwordx4 %[a12], %[b] nt\n\tglobal_load_lds_dwordx4 %[a13], %[b] offset:1024 nt\n\t" \
                         "global_load_lds_dwordx4 %[a14], %[b] offset:2048 nt\n\tglobal_load_lds_dwordx4 %[a15], %[b] offset:3072 nt\n\tds_write_b32 %[pa], %[pv]\n\ts_mov_b32 m0, %[keep]" \
                         : [keep] "=&s"(keep), [now] "=&s"(tm), [pv] "+v"(pv) \
                         : [nw] "s"(nw), [b] "s"(B), [d0] "s"(D0), [d1] "s"(D1), [pa] "v"(ctl + C_FULL * 4), [a0] "v"(O0), [a1] "v"(O1), [a2] "v"(O2), [a3] "v"(O3), [a4] "v"(O4), [a5] "v"(O5), \
                           [a6] "v"(O6), [a7] "v"(O7), [a8] "v"(O8), [a9] "v"(O9), [a10] "v"(O10), [a11] "v"(O11), [a12] "v"(O12), [a13] "v"(O13), [a14] "v"(O14), [a15] "v"(O15) \
                         : "memory", "scc"); now = rfl((unsigned)tm); gi += 4; } while (0)
#pragma unroll 1
            while (tv < NT) {
                const int lag = (int)((unsigned)now - (unsigned)t_entry) - (tref >> 4);
                const int nw = min(max(W_MAIN - 4 + (lag * PACE_K >> 8), W_MIN - 4), W_MAX - 4) & (CHAIN >= 2 ? ~3 : ~0);
                tref += 2 * PACE_B;
                if (!(sc & 1)) {
                    if (tk == 4 || tk == 6) { const int wd_ = tk == 4 ? C_WQ : C_WO; if (lds_ld(ctl + wd_ * 4) < 4) { drain(); wait_ge(wd_, 4); } }
                    needK(tk - 7);
                    const int k2_ = ks == NSLOT - 1 ? 0 : ks + 1;
                    BLOCK(kp, lbase + ks * SLOT, lbase + k2_ * SLOT, ko[0], ko[1], ko[2], ko[3], ko[4], ko[5], ko[6], ko[7], ko[8], ko[9], ko[10], ko[11], ko[12], ko[13], ko[14], ko[15]);
                    kp += 16384; ks = k2_ == NSLOT - 1 ? 0 : k2_ + 1; tk += 2;
                } else {
                    needV(tv - 7);
                    const int v2_ = vs == NSLOT - 1 ? 0 : vs + 1;
                    BLOCK(vp, lbase + vs * SLOT + 8192, lbase + v2_ * SLOT + 8192, vo[0], vo[1], vo[0], vo[1], vo[2], vo[3], vo[2], vo[3], vo[4], vo[5], vo[4], vo[5], vo[6], vo[7], vo[6], vo[7]);
                    vp += 16384; vs = v2_ == NSLOT - 1 ? 0 : v2_ + 1; tv += 2;
                }
                sc >>= 1;
            }
        }
        ST(1); STV(29, nst); STV(30, lst + 100);
        if (ffn) {
            const size_t o = (size_t)rb * 256 * 7168 + cb * 128;
            const unsigned char* gp = (const unsigned char*)wg + o;
            const unsigned char* up = (const unsigned char*)wu + o;
            const unsigned char* dp = (const unsigned char*)wd + (size_t)cb * 64 * 2048 + rb * 512;
            const unsigned v = r8 * 7168 + ch8, d = 8 * 7168 - 1024, v2 = r8 * 2048 + ch8, d2 = 8 * 2048 - 1024;
            auto issue = [&](int q) {
                if (q < 16) dma4(((q & 2) ? up : gp) + ((q >> 2) * 64 + (q & 1) * 32) * 7168, lbase + q * 4096, v, v + d, v + 2 * d, v + 3 * d);
                else dma4(dp + ((q - 16) >> 1) * 128 + (q & 1) * 32 * 2048, lbase + q * 4096, v2, v2 + d2, v2 + 2 * d2, v2 + 3 * d2);
                ++gi;
            };
            int q = 0;
#pragma unroll 1
            for (; q < PREMAX; ++q) {
                vmwait<W_MAIN - 4>(); publish((4 * gi - W_MAIN + 4) >> 2);
                if (!NOSTOP && refresh()) break;
                needV(NT - 8 + (q >> 2));
                issue(q);
            }
            STV(16, q + 1);
            if (q < 24) {
                drain();
                wait_ge(C_H1, 4);
#pragma unroll 1
                for (; q < (GATE_DN ? 16 : 24); ++q) { vmwait<W_GU - 4>(); publish((4 * gi - W_GU + 4) >> 2); issue(q); }
                ST(17);
                if (q < 24) {
                    drain();
                    ST(18);
                    wait_ge(C_H2, 4);
#pragma unroll 1
                    for (; q < 24; ++q) issue(q);
                }
            }
        }
        drain();
        ST(2);
        DUMP(ffn ? 0xE7E60070006ull : 0xE7E60000006ull);
    } else if (wave == 5) {
        if (lane < 32) *(LAS unsigned*)(L8 + L_CTL + lane * 4) = 0u;
        *(LAS v2u*)(L8 + L_ONE + lane * 8) = (v2u){0x3F803F80u, 0x3F803F80u};
        LGKM0();
        BAR();
        nap<60>();
        v4u qlo[4], qhi[4];
        wait_ge(C_QRDY, 4);
#pragma unroll
        for (int i = 0; i < 4; ++i) {
            const v4u f = *(LAS v4u*)(L8 + L_QI + (c & 7) * 272 + (4 * i + lg) * 16), z = {0, 0, 0, 0};
            qlo[i] = c < 8 ? f : z; qhi[i] = c < 8 ? z : f;
        }
        ST(3);
        unsigned ka[4];
#pragma unroll
        for (int i = 0; i < 4; ++i) ka[i] = c * 256 + (((4 * i + lg) ^ c) << 4);
        const unsigned pw = L_PF + (c & 7) * 64 + lg * 16 + (c >> 3) * 8, aw = L_PF + 1024 + (c & 7) * 4;
        float m = -INFINITY, m2 = -INFINITY, l = 0.f;
        const float scale = 0.08838834764831845f;
        int full = 0, pv = 0, slot = 4, need = G0 + 8;
#pragma unroll 1
        for (int p = 0; p < NP; ++p) {
            const int slot1 = slot == NSLOT - 1 ? 0 : slot + 1;
            LAS unsigned char* sp0 = L8 + slot * SLOT;
            LAS unsigned char* sp1 = L8 + slot1 * SLOT;
            if (p == NP - KP - 2) wait_ge(C_OK, 1);
            if (full < need - 2) full = wait_ge(C_FULL, need - 2);
            CBAR();
            v4u kf[16];
#pragma unroll
            for (int i = 0; i < 4; ++i) { kf[i] = *(LAS v4u*)(sp0 + ka[i]); kf[4 + i] = *(LAS v4u*)(sp0 + ka[i] + 4096); }
            if (full < need) full = wait_ge(C_FULL, need);
            CBAR();
#pragma unroll
            for (int i = 0; i < 4; ++i) { kf[8 + i] = *(LAS v4u*)(sp1 + ka[i]); kf[12 + i] = *(LAS v4u*)(sp1 + ka[i] + 4096); }
            lds_st(ctl + C_KDONE * 4, 2 * p + 2);
            SB();
            v4f s0 = {0, 0, 0, 0}, s1 = {0, 0, 0, 0};
#pragma unroll
            for (int i = 0; i < 4; ++i) { s0 = mfma(kf[i], qlo[i], s0); s0 = mfma(kf[4 + i], qhi[i], s0); }
#pragma unroll
            for (int i = 0; i < 4; ++i) { s1 = mfma(kf[8 + i], qlo[i], s1); s1 = mfma(kf[12 + i], qhi[i], s1); }
            float e[8];
#pragma unroll
            for (int i = 0; i < 4; ++i) {
                const v4f sv = i < 2 ? s0 : s1;
                const v2f t = (v2f){rbf(sv[2 * (i & 1)]), rbf(sv[2 * (i & 1) + 1])} * scale;
                e[2 * i] = rbf(t[0]); e[2 * i + 1] = rbf(t[1]);
            }
            m = max_over8(max3(max3(max3(e[0], e[1], m), e[2], e[3]), max3(e[4], e[5], e[6]), e[7]));
            float mn = m * LOG2E;
            asm("" : "+v"(mn));
            const float alpha = __builtin_amdgcn_exp2f(m2 - mn);
            m2 = mn;
#pragma unroll
            for (int i = 0; i < 4; ++i) {
                const v2f t = __builtin_elementwise_fma((v2f){e[2 * i], e[2 * i + 1]}, (v2f){LOG2E, LOG2E}, (v2f){-mn, -mn});
                e[2 * i] = __builtin_amdgcn_exp2f(t[0]); e[2 * i + 1] = __builtin_amdgcn_exp2f(t[1]);
            }
            if (pv < 2 * (p + 1 - FD)) pv = min(wait_ge(C_DONE0, 2 * (p + 1 - FD)), wait_ge(C_DONE1, 2 * (p + 1 - FD)));
            CBAR();
            *(LAS v2u*)(L8 + pw + (p & (FD - 1)) * FSTR) = (v2u){pack2(e[0], e[1]), pack2(e[2], e[3])};
            *(LAS v2u*)(L8 + pw + 512 + (p & (FD - 1)) * FSTR) = (v2u){pack2(e[4], e[5]), pack2(e[6], e[7])};
            *(LAS float*)(L8 + aw + (p & (FD - 1)) * FSTR) = alpha;
            lds_st(ctl + C_PRDY * 4, p + 1);
            l = l * alpha + (((e[0] + e[1]) + (e[2] + e[3])) + ((e[4] + e[5]) + (e[6] + e[7])));
            slot = slot1 == NSLOT - 1 ? 0 : slot1 + 1;
            need += p == 0 || p >= NP - KP ? 4 : 8;
        }
        l += dpp<ROR8>(l); l = swap16_add(l, l); l = swap32_add(l, l);
        if (lane < 8) st8(WS_ML + r * 128 + lane * 8, (v2u){clampw(m2), clampw(l)});
        ST(19);
        nap<DLY_ML>();
        v2u ml = {SENT, SENT};
#pragma nounroll
        for (;;) {
            CBAR();
            if (max(ml[0], ml[1]) == SENT) ml = ld8(WS_ML + (g * 64 + lane) * 128 + (head & 7) * 8);
            if (wnone(max(ml[0], ml[1]) == SENT) || spent()) break;
        }
        lds_st(ctl + C_STOP * 4, 1);
        ST(20);
        const float ms = __uint_as_float(ml[0]);
        const float f = __builtin_amdgcn_exp2f(ms - wave_max(ms));
        *(LAS float*)(L8 + L_W + lane * 4) = f / wave_sum(f * __uint_as_float(ml[1]));
        lds_st(ctl + C_WRDY * 4, 1);
        DUMP(0x9E180008u);
    } else if (wave > 5) {
        const int hf = wave - 6;
        BAR();
        unsigned va[4];
#pragma unroll
        for (int i = 0; i < 4; ++i) {
            const int row = 4 * lg + (c >> 2);
            va[i] = 8192 + row * 256 + (((4 * hf + i) ^ (row & 7)) << 5) + 8 * (c & 3);
        }
        const unsigned pr = L_PF + (c & 7) * 64 + lg * 16, ar = L_PF + 1024 + (c & 7) * 4;
        v4f acc[4];
#pragma unroll
        for (int i = 0; i < 4; ++i) acc[i] = (v4f){0, 0, 0, 0};
        int full = 0, prdy = 0, slot = 4, need = G0 + 6;
        unsigned po = WS_ACC + ((unsigned)(g * 8 + (c & 7)) * 64 + (r & 63)) * 256 + hf * 128 + (c >> 3) * 64 + lg * 16;
        PINV(po);
        nap<100>();
#pragma unroll 1
        for (int p = 0; p < NP; ++p) {
            if (prdy < p + 1) prdy = wait_ge(C_PRDY, p + 1);
            CBAR();
            const float alpha = *(LAS float*)(L8 + ar + (p & (FD - 1)) * FSTR);
            v4u pf[2];
#pragma unroll
            for (int u = 0; u < 2; ++u) pf[u] = *(LAS v4u*)(L8 + pr + u * 512 + (p & (FD - 1)) * FSTR);
#pragma unroll
            for (int i = 0; i < 4; ++i) acc[i] *= alpha;
#pragma unroll
            for (int u = 0; u < 2; ++u) {
                unsigned vb[4];
#pragma unroll
                for (int i = 0; i < 4; ++i) { vb[i] = va[i] + slot * SLOT; PINV(vb[i]); PINV(acc[i]); }
                if (full < need + 2 * u) full = wait_ge(C_FULL, need + 2 * u);
                CBAR();
                v4u vf[4];
#pragma unroll
                for (int i = 0; i < 4; ++i) vf[i] = trfrag(L8 + vb[i], 4096);
                lds_st(ctl + (C_DONE0 + hf) * 4, 2 * p + u + 1);
                SB();
#pragma unroll
                for (int i = 0; i < 4; ++i) acc[i] = mfma(vf[i], pf[u], acc[i]);
                SB();
                slot = slot == NSLOT - 1 ? 0 : slot + 1;
            }
            need += p < NP - KP - 1 ? 8 : p == NP - KP - 1 ? 4 * KP + 4 : 4;
        }
        const bool up = c >= 8;
        const v4f u0 = up ? acc[2] : acc[0], u1 = up ? acc[3] : acc[1];
        st16(po, (v4u){clampu(pack2(u0[0], u0[1])), clampu(pack2(u0[2], u0[3])), clampu(pack2(u1[0], u1[1])), clampu(pack2(u1[2], u1[3]))});
        ST(4 + hf);
        DUMP(16 << hf);
    } else {
        const int w = wave;
        const int kpos = (lane & 32) | ((lane & 12) << 1) | ((lane & 16) >> 2) | (lane & 3);
        float xres;
        {
            if (w == 0) { ST(32); STV(0, t_entry); }
            BAR();
            asm volatile("s_waitcnt vmcnt(8)" : "+v"(xa), "+v"(xb), "+v"(xm), "+v"(xr), "+v"(epw)::"memory");
            xres = bflo(xr);
            float v[16];
#pragma unroll
            for (int j = 0; j < 4; ++j) { v[2 * j] = bflo(xa[j]); v[2 * j + 1] = bfhi(xa[j]); v[8 + 2 * j] = bflo(xb[j]); v[9 + 2 * j] = bfhi(xb[j]); }
            float s = 0.f;
#pragma unroll
            for (int j = 0; j < 16; ++j) s += v[j];
            const float mean = wave_sum(s) * (1.0f / 1024);
            float q = 0.f;
#pragma unroll
            for (int j = 0; j < 16; ++j) { const float d = v[j] - mean; q += d * d; }
            const float var = wave_sum(q) * (1.0f / 1024);
            const float sd = rbf(sqrtf(rbf(rbf(var) + 1e-5f)));
            *(LAS unsigned short*)(L8 + L_XS + (w * 64 + kpos) * 2) = f2bf(rbf(bflo(xm) - rbf(mean)) / sd);
            lds_inc(ctl + C_XS * 4);
        }
        const u64 tag = ((u64)epw[0] | ((u64)epw[1] << 32)) + 0x9E3779B97F4A7C15ull;
        const v2u tg = {(unsigned)tag, (unsigned)(tag >> 32)};
        const int trow = 4 * lg + (c >> 2);
        const unsigned ta128 = trow * 128 + 8 * (c & 3), tx128 = (trow >> 1) & 3;
        {
            wait_ge(C_XS, 4);
            v4u xf[8];
#pragma unroll
            for (int k = 0; k < 8; ++k) xf[k] = *(LAS v4u*)(L8 + L_XS + k * 64 + lg * 16);
            if (w == 0) ST(39);
            VM0();
            lds_inc(ctl + C_WQL * 4);
            wait_ge(C_WQL, 4);
            if (w == 0) ST(40);
            v4u wf[8];
#pragma unroll
            for (int k = 0; k < 8; ++k) wf[k] = trfrag(L8 + k * 4096 + ta128 + ((w ^ tx128) << 5), 2048);
            lds_inc(ctl + C_WQ * 4);
            v4f d0 = {0, 0, 0, 0}, d1 = {0, 0, 0, 0};
#pragma unroll
            for (int k = 0; k < 8; k += 2) { d0 = mfma(wf[k], xf[k], d0); d1 = mfma(wf[k + 1], xf[k + 1], d1); }
            d0 += d1;
            if (c < 2) st16(WS_QP + qr * 32768 + (qc * 64 + w * 16 + lg * 4 + 2 * c) * 8, (v4u){__float_as_uint(c ? d0[2] : d0[0]), __float_as_uint(c ? d0[3] : d0[1]), tg[0], tg[1]});
            if (w == 0) ST(8);
            nap<DLY_Q>();
            v4u q[8];
            const unsigned qo = WS_QP + g * 8192 + w * 2048 + lane * 16;
#pragma unroll
            for (int j = 0; j < 8; ++j) q[j] = j >= QEXP ? (v4u){0, 0, tg[0], tg[1]} : ld16(qo + (j >> 1) * 32768 + (j & 1) * 1024);
#pragma nounroll
            for (;;) {
                bool nd[8]; u64 again = 0;
#pragma unroll
                for (int j = 0; j < 8; ++j) { nd[j] = ((q[j][2] ^ tg[0]) | (q[j][3] ^ tg[1])) != 0; again |= __builtin_amdgcn_ballot_w64(nd[j]); }
                if (again == 0 || spent()) break;
                CBAR();
#pragma unroll
                for (int j = 0; j < 8; ++j) if (nd[j]) q[j] = ld16(qo + (j >> 1) * 32768 + (j & 1) * 1024);
            }
#pragma unroll
            for (int i = 0; i < 2; ++i) {
                const v4f s = (asf(q[i]) + asf(q[2 + i])) + (asf(q[4 + i]) + asf(q[6 + i]));
                *(LAS unsigned*)(L8 + L_QI + (2 * w + i) * 272 + lane * 4) = pack2(s[0], s[1]);
            }
            lds_inc(ctl + C_QRDY * 4);
        }
        v4u wof[8];
        {
            wait_ge(C_FULL, 12);
#pragma unroll
            for (int k = 0; k < 4; ++k)
#pragma unroll
                for (int t = 0; t < 2; ++t) wof[2 * k + t] = trfrag(L8 + 2 * SLOT + k * 8192 + trow * 256 + (((2 * w + t) ^ (trow & 7)) << 5) + 8 * (c & 3), 4096);
            lds_inc(ctl + C_WO * 4);
        }
        if (w == 3) {
            const v4u sent = {SENT, SENT, SENT, SENT};
#pragma unroll
            for (int i = 0; i < 2; ++i) st16(WS_ACC + ((unsigned)(g * 8 + 4 * i + lg) * 64 + (r & 63)) * 256 + c * 16, sent);
            if (lane < 4) st16(WS_ML + r * 128 + lane * 16, sent);
            if (lane < 32) st16(WS_P + head * 4096 + ocb * 512 + lane * 16, sent);
            if (ffn) {
                st16(WS_X + (cb * 4 + rb) * XREC + lane * 16, sent);
                if (lane < 2) st16(WS_X + (cb * 4 + rb) * XREC + 1024 + lane * 16, sent);
                st16(WS_Y + cb * 4096 + rb * 1024 + lane * 16, sent);
            }
            VM0();
            if (lane == 0) st8(WS_FLAG + r * 8, tg);
            ST(6);
            bool need = true;
#pragma nounroll
            for (;;) {
                nap<DLY_F>();
                if (need) {
                    const v4u f0 = ld16(WS_FLAG + lane * 16), f1 = ld16(WS_FLAG + 1024 + lane * 16);
                    need = (((f0[0] ^ tg[0]) | (f0[1] ^ tg[1]) | (f0[2] ^ tg[0])) | ((f0[3] ^ tg[1]) | (f1[0] ^ tg[0]) | (f1[1] ^ tg[1])) | ((f1[2] ^ tg[0]) | (f1[3] ^ tg[1]))) != 0;
                }
                bud -= 256;
                if (wnone(need) || spent()) break;
            }
            if (r == 0 && lane == 0) st8(WS_EPOCH, tg);
            lds_st(ctl + C_OK * 4, 1);
            ST(7);
            DUMP(0xC0);
        }
        if (DRY && w != 3) { nap<127>(); nap<127>(); }
#pragma nounroll
        for (int pass = DRY ? 0 : 1; pass < 2; ++pass) {
            int live = pass;
            asm volatile("" : "+s"(live));
            const u64 dry = live ? 0 : 1;
            const int lim = live ? 0 : -(1 << 20);
            rs = __builtin_amdgcn_make_buffer_rsrc(ws, 0, live ? WS_END : 0u, 0x00020000);
            if (live) {
#pragma nounroll
                while (lds_ld(ctl + C_KDONE * 4) < NT && !spent()) nap<8>();
                wait_ge(C_STOP, 1); wait_ge(C_DONE0, NT);
                if (w == 0) ST(21);
            }
            {
                nap<DLY_1>();
                v4u av[4];
                const unsigned ao = WS_ACC + (head * 64 + 16 * w + 4 * lg) * 256 + c * 16;
#pragma unroll
                for (int j = 0; j < 4; ++j) av[j] = ld16(ao + j * 256);
                wait_ge(C_WRDY, live);
                const v4f w4 = *(LAS v4f*)(L8 + L_W + (16 * w + 4 * lg) * 4);
                REPOLL(4, av, ao + j * 256);
                if (w == 0) ST(9);
                v2f e[4];
#pragma unroll
                for (int i = 0; i < 4; ++i) e[i] = (v2f){bflo(av[0][i]), bfhi(av[0][i])} * w4[0];
#pragma unroll
                for (int j = 1; j < 4; ++j)
#pragma unroll
                    for (int i = 0; i < 4; ++i) e[i] += (v2f){bflo(av[j][i]), bfhi(av[j][i])} * w4[j];
                *(LAS v4u*)(L8 + L_PART + (4 * w + lg) * 272 + c * 16) = (v4u){pack2(e[0][0], e[0][1]), pack2(e[1][0], e[1][1]), pack2(e[2][0], e[2][1]), pack2(e[3][0], e[3][1])};
                lds_inc(ctl + C_H1 * 4, live);
                v4u of[4];
#pragma nounroll
                for (;;) {
                    const int n = *(volatile LAS int*)(L8 + L_CTL + C_H1 * 4);
                    CBAR();
#pragma unroll
                    for (int k2 = 0; k2 < 4; ++k2) of[k2] = *(LAS v4u*)(L8 + L_PART + c * 272 + k2 * 64 + lg * 16);
                    SB();
                    if (rfl(n) >= 4 * live || spent()) break;
                }
                v4f d0 = {0, 0, 0, 0}, d1 = {0, 0, 0, 0};
#pragma unroll
                for (int k2 = 0; k2 < 4; ++k2) { d0 = mfma(of[k2], wof[2 * k2], d0); d1 = mfma(of[k2], wof[2 * k2 + 1], d1); }
                float p = swap16_add((d0[0] + d0[1]) + (d0[2] + d0[3]), (d1[0] + d1[1]) + (d1[2] + d1[3]));
                p = swap32_add(p, p);
                if (lane < 32) st4(WS_P + head * 4096 + ocb * 512 + w * 128 + lane * 4, clampw(p));
                if (w == 0) ST(10);
            }
            if (ffn) {
                {
                    v4u gf[8], uf[8];
                    auto frags = [&](int i) {
                        wait_ge(C_FULL, GKV + 4 * i + 4 + lim);
#pragma unroll
                        for (int k2 = 2 * i; k2 < 2 * i + 2; ++k2) {
                            LAS unsigned char* sp = L8 + i * SLOT + (k2 & 1) * 4096 + ta128 + ((w ^ tx128) << 5);
                            gf[k2] = trfrag(sp, 2048); uf[k2] = trfrag(sp + 8192, 2048);
                        }
                    };
                    nap<DLY_2>();
#pragma unroll
                    for (int i = 0; i < XPOLL / 4; ++i) frags(i);
                    if (XPOLL & 3) wait_ge(C_FULL, GKV + XPOLL + lim);
                    if (w == 0) ST(22);
                    v4u pv[8];
                    const unsigned po = WS_P + lg * 4096 + rb * 1024 + w * 256 + c * 16;
#pragma unroll
                    for (int j = 0; j < 8; ++j) pv[j] = ld16(po + j * 16384);
#pragma unroll
                    for (int i = XPOLL / 4; i < 4; ++i) frags(i);
                    REPOLL(8, pv, po + j * 16384);
                    if (w == 0) ST(11);
                    const v4f e = ((asf(pv[0]) + asf(pv[1])) + (asf(pv[2]) + asf(pv[3]))) + ((asf(pv[4]) + asf(pv[5])) + (asf(pv[6]) + asf(pv[7])));
                    const float h = rbf(xres + rbf(rows_reduce4(e)));
                    const int k = 4 * c + lg;
                    *(LAS unsigned short*)(L8 + L_HS + (w * 64 + ((k & 32) | ((k & 12) << 1) | ((k & 16) >> 2) | (k & 3))) * 2) = f2bf(h);
                    lds_inc(ctl + C_H2 * 4, live);
                    v4u hf[8];
#pragma nounroll
                    for (;;) {
                        const int n = *(volatile LAS int*)(L8 + L_CTL + C_H2 * 4);
                        CBAR();
#pragma unroll
                        for (int k2 = 0; k2 < 8; ++k2) hf[k2] = *(LAS v4u*)(L8 + ((c & 1) ? L_ONE : L_HS) + k2 * 64 + lg * 16);
                        SB();
                        if (rfl(n) >= 4 * live || spent()) break;
                    }
                    const float piv = __uint_as_float(rfl(__float_as_uint(h))), dd = h - piv;
                    const float s1 = wave_sum(dd), s2 = wave_sum(dd * dd);
                    if (lane == 0) st8(WS_X + (cb * 4 + rb) * XREC + 1024 + w * 8, (v2u){clampw(s1 + 64.f * piv), clampw(s2 - s1 * s1 * (1.0f / 64))});
                    v4f dg = {0, 0, 0, 0}, du = {0, 0, 0, 0};
#pragma unroll
                    for (int k2 = 0; k2 < 8; ++k2) { dg = mfma(hf[k2], gf[k2], dg); du = mfma(hf[k2], uf[k2], du); }
                    if (lane < 16) st16(WS_X + (cb * 4 + rb) * XREC + w * 256 + lane * 16, (v4u){clampw(dg[0]), clampw(du[0]), clampw(dg[1]), clampw(du[1])});
                    if (w == 0) ST(12);
                }
                {
                    nap<DLY_3>();
                    v4u xv[5];
                    const unsigned xo = WS_X + cb * 4 * XREC + lane * 16, so = WS_X + (cb * 4 + ((c >> 1) & 3)) * XREC + 1024 + (c & 1) * 16;
#pragma unroll
                    for (int j = 0; j < 5; ++j) xv[j] = ld16(j < 4 ? xo + j * XREC : so);
                    wait_ge(C_FULL, GKV + 16 + 2 * w + 2 + lim);
                    if (w == 0) ST(23);
                    v4u df[8];
#pragma unroll
                    for (int k2 = 0; k2 < 2; ++k2)
#pragma unroll
                        for (int t = 0; t < 4; ++t) df[4 * k2 + t] = trfrag(L8 + 4 * SLOT + w * 8192 + k2 * 4096 + ta128 + ((t ^ tx128) << 5), 2048);
                    REPOLL(5, xv, j < 4 ? xo + j * XREC : so);
                    if (w == 0) ST(24);
                    const v4f sv = asf(xv[4]);
                    float tot = sv[0] + sv[2];
                    tot += dpp<ROR4>(tot); tot += dpp<ROR2>(tot); tot += dpp<ROR1>(tot);
                    const float mean = tot * (1.0f / 1024);
                    const float dm0 = sv[0] * (1.0f / 64) - mean, dm1 = sv[2] * (1.0f / 64) - mean;
                    float m2 = (sv[1] + sv[3]) + 64.f * (dm0 * dm0 + dm1 * dm1);
                    m2 += dpp<ROR4>(m2); m2 += dpp<ROR2>(m2); m2 += dpp<ROR1>(m2);
                    const float mb = rbf(mean), isd = __builtin_amdgcn_rcpf(rbf(__builtin_amdgcn_sqrtf(rbf(rbf(m2 * (1.0f / 1024)) + 1e-5f))));
                    const v4f rr = (asf(xv[0]) + asf(xv[1])) + (asf(xv[2]) + asf(xv[3]));
                    const float gg = rbf((rr[0] - mb * rr[2]) * isd), uu = rbf((rr[1] - mb * rr[3]) * isd);
                    const float ex = __builtin_amdgcn_exp2f((-2.0f * 0.7978845608028654f * 1.4426950408889634f) * (gg + 0.044715f * gg * gg * gg));
                    *(LAS unsigned short*)(L8 + L_AS + w * 128 + kpos * 2) = f2bf(rbf(gg * __builtin_amdgcn_rcpf(1.0f + ex)) * uu);
                    CBAR();
                    const v4u a0 = *(LAS v4u*)(L8 + L_AS + w * 128 + lg * 16), a1 = *(LAS v4u*)(L8 + L_AS + w * 128 + 64 + lg * 16);
                    v4f d[4];
#pragma unroll
                    for (int t = 0; t < 4; ++t) { d[t] = mfma(a0, df[t], (v4f){0, 0, 0, 0}); d[t] = mfma(a1, df[4 + t], d[t]); }
                    const float y = (lg & 2) ? ((lg & 1) ? d[3][0] : d[2][0]) : ((lg & 1) ? d[1][0] : d[0][0]);
                    st4(WS_Y + cb * 4096 + rb * 1024 + w * 256 + lane * 4, clampw(y));
                    if (w == 0) ST(13);
                }
            } else if (w == 0) {
                const __amdgpu_buffer_rsrc_t ro = __builtin_amdgcn_make_buffer_rsrc(out, 0, live ? 2048u : 0u, 0x00020000);
                const unsigned fo = (lane >> 3) * 4096 + fa * 128 + (lane & 7) * 16;
                nap<DLY_2 + 16>();
                v4u pv[4];
#pragma unroll
                for (int j = 0; j < 4; ++j) pv[j] = ld16(WS_P + fo + j * 32768);
                REPOLL(4, pv, WS_P + fo + j * 32768);
                float hs = rows_reduce4((asf(pv[0]) + asf(pv[1])) + (asf(pv[2]) + asf(pv[3])));
                hs += dpp<ROR8>(hs);
                const float h = rbf(xres + rbf(hs));
                ST(14);
                nap<DLY_4>();
                v4u yv[7];
#pragma unroll
                for (int j = 0; j < 7; ++j) yv[j] = ld16(WS_Y + fo + j * 32768);
                REPOLL(7, yv, WS_Y + fo + j * 32768);
                float ys = rows_reduce4(((asf(yv[0]) + asf(yv[1])) + (asf(yv[2]) + asf(yv[3]))) + ((asf(yv[4]) + asf(yv[5])) + asf(yv[6])));
                ys += dpp<ROR8>(ys);
                if (c < 8) __builtin_amdgcn_raw_buffer_store_b16(f2bf(h + rbf(ys)), ro, fcol * 2, 0, 0);
                ST(15);
            }
        }
        if (w == 0) DUMP(ffn ? 0x18101E03F01ull : 0x1810020C701ull);
    }
}

extern "C" void kernel_launch(void* const* d_in, const int* in_sizes, int n_in, void* d_out, int out_size, void* d_ws, size_t ws_size, hipStream_t stream) {
    (void)in_sizes; (void)n_in; (void)out_size; (void)ws_size;
    (void)hipFuncSetAttribute((const void*)fused, hipFuncAttributeMaxDynamicSharedMemorySize, L_END);
    fused<<<NWG, 512, L_END, stream>>>((const unsigned short*)d_in[0], (const unsigned short*)d_in[3], (unsigned char*)d_ws, (const unsigned short*)d_in[1],
                                       (const unsigned short*)d_in[2], (const unsigned short*)d_in[4], (const unsigned short*)d_in[6], (const unsigned short*)d_in[7],
                                       (const unsigned short*)d_in[8], (unsigned short*)d_out);
}
```

```cpp
#define PIN 0
#define PINF 0
#define DRY 0
#include <hip/hip_runtime.h>
#include <stdint.h>

typedef unsigned v4u __attribute__((ext_vector_type(4)));
typedef unsigned v2u __attribute__((ext_vector_type(2)));
typedef float v4f __attribute__((ext_vector_type(4)));
typedef float v2f __attribute__((ext_vector_type(2)));
typedef __bf16 v8bf __attribute__((ext_vector_type(8)));
typedef __bf16 v2bf __attribute__((ext_vector_type(2)));
typedef short v4s __attribute__((ext_vector_type(4)));
typedef unsigned long long u64;
#define GAS __attribute__((address_space(1)))
#define LAS __attribute__((address_space(3)))
#define RLX_AGENT __ATOMIC_RELAXED, __HIP_MEMORY_SCOPE_AGENT

#define W_MAIN 28
#define W_GU 24
#define W_DN 32
#define KP 3
#define W_HEAD 32
#define HEADG 8
#define DLY_F 100
#define NOSTOP 0
#define PREMAX 24
#define XPOLL 11
#define GATE_DN 1
#define DLY_Q 28
#define DLY_ML 16
#define DLY_1 8
#define DLY_2 4
#define DLY_3 12
#define DLY_4 50
#define PACE_A 330
#define PACE_B 443
#define PACE_K 20
#define QEXP 8
#define CHAIN 2
#define W_MIN 20
#define W_MAX 36

constexpr int NWG = 256, NT = 32;
constexpr int NSLOT = 9, SLOT = 16384, FD = 4, FSTR = 1088, NP = NT / 2;
constexpr int G0 = 8, GKV = G0 + 4 * NT;
constexpr unsigned XREC = 1152;
constexpr unsigned WS_EPOCH = 0, WS_FLAG = 4096, WS_QP = WS_FLAG + 256 * 8, WS_ACC = WS_QP + 4 * 4096 * 8, WS_ML = WS_ACC + 32 * 64 * 256,
                   WS_P = WS_ML + 256 * 128, WS_X = WS_P + 32 * 4096, WS_Y = WS_X + 224 * XREC, WS_END = WS_Y + 56 * 4096, WS_STAMP = 4u << 20;
constexpr int L_QI = NSLOT * SLOT, L_XS = L_QI + 8 * 272, L_PART = L_XS + 512, L_HS = L_PART + 16 * 272, L_ONE = L_HS + 512, L_W = L_ONE + 512, L_AS = L_W + 256,
              L_PF = L_AS + 512, L_CTL = L_PF + FD * FSTR, L_STAMP = L_CTL + 128, L_END = L_STAMP + 512;
enum { C_KDONE = 0, C_DONE0, C_DONE1, C_STOP, C_FULL, C_PRDY, C_WQ, C_WO, C_XS, C_WQL, C_QRDY, C_OK, C_WRDY, C_H1, C_H2 };
constexpr unsigned sched() { unsigned m = 0; for (int p = 0; p < NP - 1 - KP; ++p) m |= 2u << (2 * p); for (int s = 2 * (NP - 1) - KP; s < 2 * (NP - 1); ++s) m |= 1u << s; return m; }
static_assert(W_MIN >= (CHAIN == 3 ? 24 : CHAIN == 2 ? 20 : 16) && W_MAX <= 36 && (CHAIN || !PACE_K) && SLOT == 0x4000, "window");
static_assert(L_END <= 160 * 1024 && (L_XS & 15) == 0 && (L_CTL & 15) == 0 && KP < FD && (4 + NT) % NSLOT == 0, "lds");
constexpr unsigned SENT = 0xFFFFFFFFu;
constexpr int SPIN = 1 << 22;

__device__ __forceinline__ float bflo(unsigned u) { return __uint_as_float(u << 16); }
__device__ __forceinline__ float bfhi(unsigned u) { return __uint_as_float(u & 0xffff0000u); }
__device__ __forceinline__ unsigned short f2bf(float f) { __bf16 b = (__bf16)f; return __builtin_bit_cast(unsigned short, b); }
__device__ __forceinline__ unsigned pack2(float a, float b) { return __builtin_bit_cast(unsigned, __builtin_convertvector((v2f){a, b}, v2bf)); }
__device__ __forceinline__ float rbf(float f) { return __uint_as_float(pack2(0.f, f)); }
__device__ __forceinline__ unsigned clampu(unsigned u) { return u < 0xFFFFFFFEu ? u : 0xFFFFFFFEu; }
__device__ __forceinline__ unsigned clampw(float f) { return clampu(__float_as_uint(f)); }
__device__ __forceinline__ unsigned umax4(v4u v) { return max(max(v[0], v[1]), max(v[2], v[3])); }
__device__ __forceinline__ v4f asf(v4u v) { return __builtin_bit_cast(v4f, v); }

template <int CTRL> __device__ __forceinline__ float dpp(float v) {
    return __builtin_bit_cast(float, __builtin_amdgcn_update_dpp(0, __builtin_bit_cast(int, v), CTRL, 0xf, 0xf, true));
}
constexpr int ROR1 = 0x121, ROR2 = 0x122, ROR4 = 0x124, ROR8 = 0x128;
__device__ __forceinline__ float row_sum(float v) { v += dpp<ROR8>(v); v += dpp<ROR4>(v); v += dpp<ROR2>(v); v += dpp<ROR1>(v); return v; }
__device__ __forceinline__ float row_max(float v) { v = fmaxf(v, dpp<ROR8>(v)); v = fmaxf(v, dpp<ROR4>(v)); v = fmaxf(v, dpp<ROR2>(v)); v = fmaxf(v, dpp<ROR1>(v)); return v; }
__device__ __forceinline__ float swap16_add(float a, float b) {
    const auto r = __builtin_amdgcn_permlane16_swap(__float_as_uint(a), __float_as_uint(b), false, false);
    return __uint_as_float(r[0]) + __uint_as_float(r[1]);
}
__device__ __forceinline__ float swap32_add(float a, float b) {
    const auto r = __builtin_amdgcn_permlane32_swap(__float_as_uint(a), __float_as_uint(b), false, false);
    return __uint_as_float(r[0]) + __uint_as_float(r[1]);
}
__device__ __forceinline__ float x16_max(float a) {
    const auto r = __builtin_amdgcn_permlane16_swap(__float_as_uint(a), __float_as_uint(a), false, false);
    return fmaxf(__uint_as_float(r[0]), __uint_as_float(r[1]));
}
__device__ __forceinline__ float x32_max(float a) {
    const auto r = __builtin_amdgcn_permlane32_swap(__float_as_uint(a), __float_as_uint(a), false, false);
    return fmaxf(__uint_as_float(r[0]), __uint_as_float(r[1]));
}
__device__ __forceinline__ float wave_sum(float v) { v = row_sum(v); v = swap16_add(v, v); return swap32_add(v, v); }
__device__ __forceinline__ float wave_max(float v) { return x32_max(x16_max(row_max(v))); }
__device__ __forceinline__ float rows_reduce4(v4f v) { return swap32_add(swap16_add(v[0], v[1]), swap16_add(v[2], v[3])); }

__device__ __forceinline__ v4f mfma(v4u a, v4u b, v4f c) {
    return __builtin_amdgcn_mfma_f32_16x16x32_bf16(__builtin_bit_cast(v8bf, a), __builtin_bit_cast(v8bf, b), c, 0, 0, 0);
}
__device__ __forceinline__ v2u trread(LAS unsigned char* p) {
    return __builtin_bit_cast(v2u, __builtin_amdgcn_ds_read_tr16_b64_v4i16((LAS v4s*)p));
}
__device__ __forceinline__ v4u trfrag(LAS unsigned char* p, int hi_off) { const v2u a = trread(p), b = trread(p + hi_off); return (v4u){a[0], a[1], b[0], b[1]}; }

#define CBAR() asm volatile("" ::: "memory")
#define LGKM0() asm volatile("s_waitcnt lgkmcnt(0)" ::: "memory")
#define VM0() asm volatile("s_waitcnt vmcnt(0)" ::: "memory")
#define BAR() asm volatile("s_barrier" ::: "memory")
#define SB() __builtin_amdgcn_sched_barrier(0)
#define PINV(x) asm volatile("" : "+v"(x))
template <int N> __device__ __forceinline__ void vmwait() { asm volatile("s_waitcnt vmcnt(%0)" ::"n"(N) : "memory"); }
template <int N> __device__ __forceinline__ void nap() { if (N > 0) asm volatile("s_sleep %0" ::"n"(N) : "memory"); }

__device__ __forceinline__ void dma4(const void* sbase, unsigned dst, unsigned v0, unsigned v1, unsigned v2, unsigned v3) {
    unsigned keep;
    asm volatile("s_mov_b32 %0, m0\n\ts_mov_b32 m0, %2\n\ts_nop 0\n\t"
                 "global_load_lds_dwordx4 %3, %1 nt\n\tglobal_load_lds_dwordx4 %4, %1 offset:1024 nt\n\t"
                 "global_load_lds_dwordx4 %5, %1 offset:2048 nt\n\tglobal_load_lds_dwordx4 %6, %1 offset:3072 nt\n\ts_mov_b32 m0, %0"
                 : "=&s"(keep) : "s"(sbase), "s"(dst), "v"(v0), "v"(v1), "v"(v2), "v"(v3) : "memory");
}
__device__ __forceinline__ int lds_ld(unsigned addr) {
    unsigned v;
    asm volatile("ds_read_b32 %0, %1\n\ts_waitcnt lgkmcnt(0)" : "=&v"(v) : "v"(addr) : "memory");
    return __builtin_amdgcn_readfirstlane(v);
}
__device__ __forceinline__ v4u lds_ld4(unsigned addr) {
    v4u v;
    asm volatile("ds_read_b128 %0, %1\n\ts_waitcnt lgkmcnt(0)" : "=&v"(v) : "v"(addr) : "memory");
    return v;
}
__device__ __forceinline__ void lds_st(unsigned addr, int v) { asm volatile("ds_write_b32 %0, %1" ::"v"(addr), "v"(v) : "memory"); }
__device__ __forceinline__ void lds_inc(unsigned addr, int n = 1) { asm volatile("s_mov_b64 exec, 1\n\tds_add_u32 %0, %1\n\ts_mov_b64 exec, -1" ::"v"(addr), "v"(n) : "memory"); }
__device__ __forceinline__ float max3(float a, float b, float c) { float d; asm("v_max3_f32 %0, %1, %2, %3" : "=v"(d) : "v"(a), "v"(b), "v"(c)); return d; }
__device__ __forceinline__ float max_over8(float a) {
    float t;
    asm("s_nop 1\n\tv_max_f32_dpp %0, %0, %0 row_ror:8 row_mask:0xf bank_mask:0xf\n\tv_mov_b32 %1, %0\n\ts_nop 1\n\tv_permlane16_swap_b32 %0, %1\n\ts_nop 0\n\tv_max_f32 %0, %0, %1\n\t"
        "v_mov_b32 %1, %0\n\ts_nop 1\n\tv_permlane32_swap_b32 %0, %1\n\ts_nop 0\n\tv_max_f32 %0, %0, %1" : "+v"(a), "=&v"(t));
    return a;
}
__device__ __forceinline__ int rfl(unsigned v) { return __builtin_amdgcn_readfirstlane(v); }

__device__ __forceinline__ bool wnone(bool p) { return __builtin_amdgcn_ballot_w64(p) == 0ull; }

#define STAMP
#define ST(i) do { if (lane == 0) *(volatile LAS u64*)(L8 + L_STAMP + (i) * 8) = __builtin_amdgcn_s_memrealtime(); } while (0)
#define STV(i, v) do { if (lane == 0) *(volatile LAS u64*)(L8 + L_STAMP + (i) * 8) = (v); } while (0)
#define DUMP(mask) do {} while (0)

__global__ void __launch_bounds__(512, 2) fused(const unsigned short* __restrict__ x, const unsigned short* __restrict__ wq, unsigned char* __restrict__ ws,
                                                const unsigned short* __restrict__ Kc, const unsigned short* __restrict__ Vc, const unsigned short* __restrict__ wo,
                                                const unsigned short* __restrict__ wg, const unsigned short* __restrict__ wu, const unsigned short* __restrict__ wd,
                                                unsigned short* __restrict__ out) {
    const u64 t_entry = __builtin_amdgcn_s_memrealtime();
    extern __shared__ __attribute__((aligned(16))) unsigned char smem[];
    LAS unsigned char* L8 = (LAS unsigned char*)smem;
    const unsigned lbase = (unsigned)(uintptr_t)L8;
    const int lane = threadIdx.x & 63, wave = __builtin_amdgcn_readfirstlane(threadIdx.x >> 6), b = blockIdx.x;
    const int c = lane & 15, lg = lane >> 4;
    const int xc = b & 7, jj = b >> 3, r = xc * 32 + jj;
    const int g = r >> 6, head = r >> 3, ocb = r & 7;
    const int qr = (r >> 4) & 3, qc = g * 16 + (r & 15);
    const bool ffn = jj < 28;
    const int rb = xc >> 1, cb = (xc & 1) * 28 + jj;
    const unsigned ctl = lbase + L_CTL;
    const unsigned r8 = lane >> 3, ch8 = ((lane & 7) ^ (((r8 >> 1) & 3) << 1)) << 4;
    const int fa = xc * 4 + jj - 28, fcol = fa * 32 + 4 * (lane & 7) + lg;
    v4u xa, xb; v2u epw; unsigned xm, xr;
    {
        const unsigned v = (qr * 256 + (wave & 3) * 64 + r8) * 8192 + qc * 128 + ch8, d = 8 * 8192 - 1024, u = v + 32 * 8192;
        unsigned keep;
        asm volatile("s_cmp_gt_i32 %[wv], 3\n\ts_cbranch_scc1 .Lskip%=\n\t"
                     "global_load_dwordx4 %[xa], %[o0], %[x]\n\tglobal_load_dwordx4 %[xb], %[o0], %[x] offset:16\n\t"
                     "global_load_ushort %[xm], %[o1], %[x]\n\tglobal_load_ushort %[xr], %[o2], %[x]\n\tglobal_load_dwordx2 %[ep], %[z], %[ws] sc1\n\t"
                     "s_mov_b32 %[keep], m0\n\ts_mov_b32 m0, %[d0]\n\ts_nop 0\n\t"
                     "global_load_lds_dwordx4 %[v0], %[b0] nt\n\tglobal_load_lds_dwordx4 %[v1], %[b0] offset:1024 nt\n\t"
                     "global_load_lds_dwordx4 %[v2], %[b0] offset:2048 nt\n\tglobal_load_lds_dwordx4 %[v3], %[b0] offset:3072 nt\n\t"
                     "s_mov_b32 m0, %[d1]\n\ts_nop 0\n\t"
                     "global_load_lds_dwordx4 %[u0], %[b0] nt\n\tglobal_load_lds_dwordx4 %[u1], %[b0] offset:1024 nt\n\t"
                     "global_load_lds_dwordx4 %[u2], %[b0] offset:2048 nt\n\tglobal_load_lds_dwordx4 %[u3], %[b0] offset:3072 nt\n\t"
                     "s_mov_b32 m0, %[keep]\n.Lskip%=:"
                     : [xa] "=&v"(xa), [xb] "=&v"(xb), [xm] "=&v"(xm), [xr] "=&v"(xr), [ep] "=&v"(epw), [keep] "=&s"(keep)
                     : [wv] "s"(wave), [x] "s"(x), [ws] "s"(ws), [z] "v"(WS_EPOCH), [o0] "v"(lane * 32), [o1] "v"((qr * 256 + (wave & 3) * 64 + lane) * 2),
                       [o2] "v"((ffn ? rb * 256 + (wave & 3) * 64 + 4 * c + lg : fcol) * 2), [b0] "s"(wq), [d0] "s"(lbase + wave * 8192), [d1] "s"(lbase + wave * 8192 + 4096),
                       [v0] "v"(v), [v1] "v"(v + d), [v2] "v"(v + 2 * d), [v3] "v"(v + 3 * d), [u0] "v"(u), [u1] "v"(u + d), [u2] "v"(u + 2 * d), [u3] "v"(u + 3 * d)
                     : "memory", "scc");
    }
    const unsigned vv0 = lg * 256 + ((c ^ (lg << 1)) << 4), vv1 = lg * 256 + ((c ^ ((4 + lg) << 1)) << 4);
    const unsigned k0 = lg * 256 + ((c ^ lg) << 4), k1 = k0 ^ 64, k2 = k0 ^ 128, k3 = k0 ^ 192;
    const unsigned char* kp = (const unsigned char*)Kc + (size_t)r * NT * 8192;
    const unsigned char* vp = (const unsigned char*)Vc + (size_t)r * NT * 8192;
    const unsigned char* op = (const unsigned char*)wo + (size_t)head * 128 * 2048 + ocb * 256;
    const unsigned o0 = vv0 + lg * (2048 - 256), o1 = vv1 + lg * (2048 - 256) + 7168, o2 = o0 + 2 * 7168, o3 = o1 + 2 * 7168;
    {
#define G4(B, A0, A1, A2, A3) "s_nop 0\n\tglobal_load_lds_dwordx4 %[" #A0 "], %[" #B "] nt\n\tglobal_load_lds_dwordx4 %[" #A1 "], %[" #B "] offset:1024 nt\n\t" \
                              "global_load_lds_dwordx4 %[" #A2 "], %[" #B "] offset:2048 nt\n\tglobal_load_lds_dwordx4 %[" #A3 "], %[" #B "] offset:3072 nt\n\t"
        unsigned keep;
        asm volatile("s_cmp_lg_i32 %[wv], 4\n\ts_cbranch_scc1 .Lskiq%=\n\ts_mov_b32 %[keep], m0\n\t"
                     "s_add_i32 m0, %[lb], 0x10000\n\t" G4(kp, k0, k1, k2, k3) "s_add_i32 m0, %[lb], 0x12000\n\t" G4(vp, v0, v1, v0, v1)
                     "s_add_i32 m0, %[lb], 0x8000\n\t" G4(op, o0, o1, o2, o3)
                     "s_add_i32 m0, %[lb], 0x11000\n\t" G4(kq, k0, k1, k2, k3) "s_add_i32 m0, %[lb], 0x13000\n\t" G4(vq, v0, v1, v0, v1)
                     "s_add_i32 m0, %[lb], 0x9000\n\t" G4(p1, o0, o1, o2, o3)
                     "s_add_i32 m0, %[lb], 0xa000\n\t" G4(p2, o0, o1, o2, o3) "s_add_i32 m0, %[lb], 0xb000\n\t" G4(p3, o0, o1, o2, o3)
                     "s_mov_b32 m0, %[keep]\n.Lskiq%=:"
                     : [keep] "=&s"(keep)
                     : [wv] "s"(wave), [lb] "s"(lbase), [kp] "s"(kp), [vp] "s"(vp), [op] "s"(op), [kq] "s"(kp + 4096), [vq] "s"(vp + 4096), [p1] "s"(op + 32768), [p2] "s"(op + 65536),
                       [p3] "s"(op + 98304), [k0] "v"(k0), [k1] "v"(k1), [k2] "v"(k2), [k3] "v"(k3), [v0] "v"(vv0), [v1] "v"(vv1), [o0] "v"(o0), [o1] "v"(o1), [o2] "v"(o2), [o3] "v"(o3)
                     : "memory", "scc");
    }
    asm volatile("" ::"s"(wg), "s"(wu));
    __amdgpu_buffer_rsrc_t rs = __builtin_amdgcn_make_buffer_rsrc(ws, 0, WS_END, 0x00020000);
    const float LOG2E = 1.4426950408889634f;
    int bud = SPIN;
    auto wait_ge = [&](int word, int need) -> int {
        int v; unsigned t;
        bud = __builtin_amdgcn_readfirstlane(bud); need = __builtin_amdgcn_readfirstlane(need);
        asm volatile(".Lw%=:\n\tds_read_b32 %1, %3\n\ts_waitcnt lgkmcnt(0)\n\tv_readfirstlane_b32 %0, %1\n\ts_cmp_ge_i32 %0, %4\n\ts_cbranch_scc1 .Ld%=\n\t"
                     "s_sub_i32 %2, %2, 1\n\ts_cmp_ge_i32 %2, 0\n\ts_cbranch_scc1 .Lw%=\n.Ld%=:"
                     : "=&s"(v), "=&v"(t), "+s"(bud) : "v"(ctl + word * 4), "s"(need) : "memory", "scc");
        return v;
    };
    auto spent = [&]() -> bool { bud -= 8; return bud < 0; };
    auto ld16 = [&](unsigned off) -> v4u { return __builtin_amdgcn_raw_buffer_load_b128(rs, off, 0, 16); };
    auto ld8 = [&](unsigned off) -> v2u { return __builtin_amdgcn_raw_buffer_load_b64(rs, off, 0, 16); };
    auto st16 = [&](unsigned off, v4u v) { __builtin_amdgcn_raw_buffer_store_b128(v, rs, off, 0, 16); };
    auto st8 = [&](unsigned off, v2u v) { __builtin_amdgcn_raw_buffer_store_b64(v, rs, off, 0, 16); };
    auto st4 = [&](unsigned off, unsigned v) { __builtin_amdgcn_raw_buffer_store_b32(v, rs, off, 0, 16); };
#define REPOLL(N, V, OFF) do { u64 again = dry; _Pragma("nounroll") for (;;) { bool nd[N]; \
                _Pragma("unroll") for (int j = 0; j < N; ++j) { nd[j] = umax4(V[j]) == SENT; again |= __builtin_amdgcn_ballot_w64(nd[j]); } \
                if (again == 0) break; \
                if (spent()) break; \
                again = 0; CBAR(); \
                _Pragma("unroll") for (int j = 0; j < N; ++j) if (nd[j]) V[j] = ld16(OFF); } } while (0)

    if (wave == 4) {
        ST(38);
        int gi = 0;
        int pub = 0;
        auto publish = [&](int v) { pub = max(pub, v); lds_st(ctl + C_FULL * 4, pub); };
        auto drain = [&]() {
            vmwait<36>(); publish(gi - 9); vmwait<32>(); publish(gi - 8); vmwait<28>(); publish(gi - 7); vmwait<24>(); publish(gi - 6); vmwait<20>(); publish(gi - 5); vmwait<16>(); publish(gi - 4);
            vmwait<12>(); publish(gi - 3); vmwait<8>(); publish(gi - 2); vmwait<4>(); publish(gi - 1); vmwait<0>(); publish(gi);
        };
        int kseen = 0, vseen = 0;
        auto refresh = [&]() -> int { const v4u cw = lds_ld4(ctl); kseen = rfl(cw[0]); vseen = min(rfl(cw[1]), rfl(cw[2])); return rfl(cw[3]); };
        int nst = 1, lst = 0;
        auto needK = [&](int n) { if (kseen < n) { refresh(); if (kseen < n) { ++nst; lst = n; drain(); kseen = wait_ge(C_KDONE, n); } } };
        auto needV = [&](int n) { if (vseen < n) { refresh(); if (vseen < n) { ++nst; lst = n; drain(); vseen = min(wait_ge(C_DONE0, n), wait_ge(C_DONE1, n)); } } };
#define GROUP(W, SB, DST, V0, V1, V2, V3) do { vmwait<(W) - 4>(); publish((4 * gi - (W) + 4) >> 2); dma4(SB, DST, V0, V1, V2, V3); ++gi; } while (0)
        gi = HEADG;
        BAR();
#pragma unroll 1
        for (int i = HEADG - 4; i < 8; ++i) GROUP(W_HEAD, op + (size_t)i * 16 * 2048, lbase + 2 * SLOT + i * 4096, o0, o1, o2, o3);
        ST(33);
        kp += 8192; vp += 8192;
        GROUP(W_HEAD, kp, lbase + 5 * SLOT, k0, k1, k2, k3); GROUP(W_HEAD, kp + 4096, lbase + 5 * SLOT + 4096, k0, k1, k2, k3);
        GROUP(W_HEAD, vp, lbase + 5 * SLOT + 8192, vv0, vv1, vv0, vv1); GROUP(W_HEAD, vp + 4096, lbase + 5 * SLOT + 12288, vv0, vv1, vv0, vv1);
        kp += 8192; vp += 8192;
        {
            int ks = 6, vs = 6, tk = 2, tv = 2;
            unsigned sc = sched();
            int tref = PACE_A * 16 + 2 * PACE_B;
            unsigned now = (unsigned)__builtin_amdgcn_s_memrealtime();
            unsigned ko[16], vo[8];
#pragma unroll
            for (int i = 0; i < 4; ++i) { ko[4 * i] = k0 + 4096 * i; ko[4 * i + 1] = k1 + 4096 * i; ko[4 * i + 2] = k2 + 4096 * i; ko[4 * i + 3] = k3 + 4096 * i; vo[2 * i] = vv0 + 4096 * i; vo[2 * i + 1] = vv1 + 4096 * i; }
#define LINK(N, L) "s_cmp_ge_i32 %[nw], " #N "\n\ts_waitcnt vmcnt(" #N ")\n\ts_cbranch_scc1 ." #L "%=\n\t"
#define STR2(x) #x
#define STR(x) STR2(x)
#define WAITV(L) LINK(32, L) LINK(28, L) LINK(24, L) LINK(20, L) "s_waitcnt vmcnt(16)\n." #L "%=:\n\t"
#define G4V(L, A0, A1, A2, A3) WAITV(L) "global_load_lds_dwordx4 %[" #A0 "], %[b] nt\n\tglobal_load_lds_dwordx4 %[" #A1 "], %[b] offset:1024 nt\n\t" \
            "global_load_lds_dwordx4 %[" #A2 "], %[b] offset:2048 nt\n\tglobal_load_lds_dwordx4 %[" #A3 "], %[b] offset:3072 nt\n\tds_write_b32 %[pa], %[pv]\n\tv_add_u32 %[pv], 1, %[pv]\n\t"
#define BLOCK(B, D0, D1, O0, O1, O2, O3, O4, O5, O6, O7, O8, O9, O10, O11, O12, O13, O14, O15) do { unsigned keep, pv = gi - ((nw + 3) >> 2); u64 tm; \
            asm volatile("s_mov_b32 %[keep], m0\n\ts_mov_b32 m0, %[d0]\n\t" G4V(La, a0, a1, a2, a3) "s_add_i32 m0, %[d0], 0x1000\n\t" G4V(Lb, a4, a5, a6, a7) \
                         "s_memrealtime %[now]\n\ts_mov_b32 m0, %[d1]\n\t" G4V(Lc, a8, a9, a10, a11) "s_add_i32 m0, %[d1], 0x1000\n\t" WAITV(Ld) "s_waitcnt lgkmcnt(0)\n\t" \
                         "global_load_lds_dwordx4 %[a12], %[b] nt\n\tglobal_load_lds_dwordx4 %[a13], %[b] offset:1024 nt\n\t" \
                         "global_load_lds_dwordx4 %[a14], %[b] offset:2048 nt\n\tglobal_load_lds_dwordx4 %[a15], %[b] offset:3072 nt\n\tds_write_b32 %[pa], %[pv]\n\ts_mov_b32 m0, %[keep]" \
                         : [keep] "=&s"(keep), [now] "=&s"(tm), [pv] "+v"(pv) \
                         : [nw] "s"(nw), [b] "s"(B), [d0] "s"(D0), [d1] "s"(D1), [pa] "v"(ctl + C_FULL * 4), [a0] "v"(O0), [a1] "v"(O1), [a2] "v"(O2), [a3] "v"(O3), [a4] "v"(O4), [a5] "v"(O5), \
                           [a6] "v"(O6), [a7] "v"(O7), [a8] "v"(O8), [a9] "v"(O9), [a10] "v"(O10), [a11] "v"(O11), [a12] "v"(O12), [a13] "v"(O13), [a14] "v"(O14), [a15] "v"(O15) \
                         : "memory", "scc"); now = rfl((unsigned)tm); gi += 4; } while (0)
#pragma unroll 1
            while (tv < NT) {
                const int lag = (int)((unsigned)now - (unsigned)t_entry) - (tref >> 4);
                const int nw = min(max(W_MAIN - 4 + (lag * PACE_K >> 8), W_MIN - 4), W_MAX - 4) & (CHAIN >= 2 ? ~3 : ~0);
                tref += 2 * PACE_B;
                if (!(sc & 1)) {
                    if (tk == 4) ST(34); if (tk == 8) { ST(35); STV(41, nw + 4); } if (tk == 16) { ST(36); STV(42, nw + 4); } if (tk == 24) { ST(37); STV(43, nw + 4); }
                    if (tk == 4 || tk == 6) { const int wd_ = tk == 4 ? C_WQ : C_WO; if (lds_ld(ctl + wd_ * 4) < 4) { drain(); wait_ge(wd_, 4); } }
                    needK(tk - 7);
                    const int k2_ = ks == NSLOT - 1 ? 0 : ks + 1;
                    BLOCK(kp, lbase + ks * SLOT, lbase + k2_ * SLOT, ko[0], ko[1], ko[2], ko[3], ko[4], ko[5], ko[6], ko[7], ko[8], ko[9], ko[10], ko[11], ko[12], ko[13], ko[14], ko[15]);
                    kp += 16384; ks = k2_ == NSLOT - 1 ? 0 : k2_ + 1; tk += 2;
                } else {
                    needV(tv - 7);
                    const int v2_ = vs == NSLOT - 1 ? 0 : vs + 1;
                    BLOCK(vp, lbase + vs * SLOT + 8192, lbase + v2_ * SLOT + 8192, vo[0], vo[1], vo[0], vo[1], vo[2], vo[3], vo[2], vo[3], vo[4], vo[5], vo[4], vo[5], vo[6], vo[7], vo[6], vo[7]);
                    vp += 16384; vs = v2_ == NSLOT - 1 ? 0 : v2_ + 1; tv += 2;
                }
                sc >>= 1;
            }
        }
        ST(1); STV(29, nst); STV(30, lst + 100);
        if (ffn) {
            const size_t o = (size_t)rb * 256 * 7168 + cb * 128;
            const unsigned char* gp = (const unsigned char*)wg + o;
            const unsigned char* up = (const unsigned char*)wu + o;
            const unsigned char* dp = (const unsigned char*)wd + (size_t)cb * 64 * 2048 + rb * 512;
            const unsigned v = r8 * 7168 + ch8, d = 8 * 7168 - 1024, v2 = r8 * 2048 + ch8, d2 = 8 * 2048 - 1024;
            auto issue = [&](int q) {
                if (q < 16) dma4(((q & 2) ? up : gp) + ((q >> 2) * 64 + (q & 1) * 32) * 7168, lbase + q * 4096, v, v + d, v + 2 * d, v + 3 * d);
                else dma4(dp + ((q - 16) >> 1) * 128 + (q & 1) * 32 * 2048, lbase + q * 4096, v2, v2 + d2, v2 + 2 * d2, v2 + 3 * d2);
                ++gi;
            };
            int q = 0;
#pragma unroll 1
            for (; q < PREMAX; ++q) {
                vmwait<W_MAIN - 4>(); publish((4 * gi - W_MAIN + 4) >> 2);
                if (!NOSTOP && refresh()) break;
                needV(NT - 8 + (q >> 2));
                issue(q);
            }
            STV(16, q + 1);
            if (q < 24) {
                drain();
                wait_ge(C_H1, 4);
#pragma unroll 1
                for (; q < (GATE_DN ? 16 : 24); ++q) { vmwait<W_GU - 4>(); publish((4 * gi - W_GU + 4) >> 2); issue(q); }
                ST(17);
                if (q < 24) {
                    drain();
                    ST(18);
                    wait_ge(C_H2, 4);
#pragma unroll 1
                    for (; q < 24; ++q) issue(q);
                }
            }
        }
        drain();
        ST(2);
        DUMP(ffn ? 0xE7E60070006ull : 0xE7E60000006ull);
    } else if (wave == 5) {
        if (lane < 32) *(LAS unsigned*)(L8 + L_CTL + lane * 4) = 0u;
        *(LAS v2u*)(L8 + L_ONE + lane * 8) = (v2u){0x3F803F80u, 0x3F803F80u};
        LGKM0();
        BAR();
        nap<60>();
        v4u qlo[4], qhi[4];
        wait_ge(C_QRDY, 4);
#pragma unroll
        for (int i = 0; i < 4; ++i) {
            const v4u f = *(LAS v4u*)(L8 + L_QI + (c & 7) * 272 + (4 * i + lg) * 16), z = {0, 0, 0, 0};
            qlo[i] = c < 8 ? f : z; qhi[i] = c < 8 ? z : f;
        }
        ST(3);
        unsigned ka[4];
#pragma unroll
        for (int i = 0; i < 4; ++i) ka[i] = c * 256 + (((4 * i + lg) ^ c) << 4);
        const unsigned pw = L_PF + (c & 7) * 64 + lg * 16 + (c >> 3) * 8, aw = L_PF + 1024 + (c & 7) * 4;
        float m = -INFINITY, m2 = -INFINITY, l = 0.f;
        const float scale = 0.08838834764831845f;
        int full = 0, pv = 0, slot = 4, need = G0 + 8;
        u64 cyc0 = 0;
#pragma unroll 1
        for (int p = 0; p < NP; ++p) {
            const int slot1 = slot == NSLOT - 1 ? 0 : slot + 1;
            LAS unsigned char* sp0 = L8 + slot * SLOT;
            LAS unsigned char* sp1 = L8 + slot1 * SLOT;
            if (p == NP - KP - 2) wait_ge(C_OK, 1);
            if (full < need - 2) full = wait_ge(C_FULL, need - 2);
            CBAR();
            v4u kf[16];
#pragma unroll
            for (int i = 0; i < 4; ++i) { kf[i] = *(LAS v4u*)(sp0 + ka[i]); kf[4 + i] = *(LAS v4u*)(sp0 + ka[i] + 4096); }
            if (full < need) full = wait_ge(C_FULL, need);
            CBAR();
#pragma unroll
            for (int i = 0; i < 4; ++i) { kf[8 + i] = *(LAS v4u*)(sp1 + ka[i]); kf[12 + i] = *(LAS v4u*)(sp1 + ka[i] + 4096); }
            lds_st(ctl + C_KDONE * 4, 2 * p + 2);
            SB();
            v4f s0 = {0, 0, 0, 0}, s1 = {0, 0, 0, 0};
#pragma unroll
            for (int i = 0; i < 4; ++i) { s0 = mfma(kf[i], qlo[i], s0); s0 = mfma(kf[4 + i], qhi[i], s0); }
#pragma unroll
            for (int i = 0; i < 4; ++i) { s1 = mfma(kf[8 + i], qlo[i], s1); s1 = mfma(kf[12 + i], qhi[i], s1); }
            float e[8];
#pragma unroll
            for (int i = 0; i < 4; ++i) {
                const v4f sv = i < 2 ? s0 : s1;
                const v2f t = (v2f){rbf(sv[2 * (i & 1)]), rbf(sv[2 * (i & 1) + 1])} * scale;
                e[2 * i] = rbf(t[0]); e[2 * i + 1] = rbf(t[1]);
            }
            m = max_over8(max3(max3(max3(e[0], e[1], m), e[2], e[3]), max3(e[4], e[5], e[6]), e[7]));
            float mn = m * LOG2E;
            asm("" : "+v"(mn));
            const float alpha = __builtin_amdgcn_exp2f(m2 - mn);
            m2 = mn;
#pragma unroll
            for (int i = 0; i < 4; ++i) {
                const v2f t = __builtin_elementwise_fma((v2f){e[2 * i], e[2 * i + 1]}, (v2f){LOG2E, LOG2E}, (v2f){-mn, -mn});
                e[2 * i] = __builtin_amdgcn_exp2f(t[0]); e[2 * i + 1] = __builtin_amdgcn_exp2f(t[1]);
            }
            if (pv < 2 * (p + 1 - FD)) pv = min(wait_ge(C_DONE0, 2 * (p + 1 - FD)), wait_ge(C_DONE1, 2 * (p + 1 - FD)));
            CBAR();
            *(LAS v2u*)(L8 + pw + (p & (FD - 1)) * FSTR) = (v2u){pack2(e[0], e[1]), pack2(e[2], e[3])};
            *(LAS v2u*)(L8 + pw + 512 + (p & (FD - 1)) * FSTR) = (v2u){pack2(e[4], e[5]), pack2(e[6], e[7])};
            *(LAS float*)(L8 + aw + (p & (FD - 1)) * FSTR) = alpha;
            lds_st(ctl + C_PRDY * 4, p + 1);
            l = l * alpha + (((e[0] + e[1]) + (e[2] + e[3])) + ((e[4] + e[5]) + (e[6] + e[7])));
            if (p == 0) { ST(25); cyc0 = __builtin_amdgcn_s_memtime(); } if (p == 1) STV(31, __builtin_amdgcn_s_memtime() - cyc0); if (p == 3) ST(26); if (p == 7) ST(27); if (p == 11) ST(28);
            slot = slot1 == NSLOT - 1 ? 0 : slot1 + 1;
            need += p == 0 || p >= NP - KP ? 4 : 8;
        }
        l += dpp<ROR8>(l); l = swap16_add(l, l); l = swap32_add(l, l);
        if (lane < 8) st8(WS_ML + r * 128 + lane * 8, (v2u){clampw(m2), clampw(l)});
        ST(19);
        nap<DLY_ML>();
        v2u ml = {SENT, SENT};
#pragma nounroll
        for (;;) {
            CBAR();
            if (max(ml[0], ml[1]) == SENT) ml = ld8(WS_ML + (g * 64 + lane) * 128 + (head & 7) * 8);
            if (wnone(max(ml[0], ml[1]) == SENT) || spent()) break;
        }
        lds_st(ctl + C_STOP * 4, 1);
        ST(20);
        const float ms = __uint_as_float(ml[0]);
        const float f = __builtin_amdgcn_exp2f(ms - wave_max(ms));
        *(LAS float*)(L8 + L_W + lane * 4) = f / wave_sum(f * __uint_as_float(ml[1]));
        lds_st(ctl + C_WRDY * 4, 1);
        DUMP(0x9E180008u);
    } else if (wave > 5) {
        const int hf = wave - 6;
        BAR();
        unsigned va[4];
#pragma unroll
        for (int i = 0; i < 4; ++i) {
            const int row = 4 * lg + (c >> 2);
            va[i] = 8192 + row * 256 + (((4 * hf + i) ^ (row & 7)) << 5) + 8 * (c & 3);
        }
        const unsigned pr = L_PF + (c & 7) * 64 + lg * 16, ar = L_PF + 1024 + (c & 7) * 4;
        v4f acc[4];
#pragma unroll
        for (int i = 0; i < 4; ++i) acc[i] = (v4f){0, 0, 0, 0};
        int full = 0, prdy = 0, slot = 4, need = G0 + 6;
        unsigned po = WS_ACC + ((unsigned)(g * 8 + (c & 7)) * 64 + (r & 63)) * 256 + hf * 128 + (c >> 3) * 64 + lg * 16;
        PINV(po);
        nap<100>();
#pragma unroll 1
        for (int p = 0; p < NP; ++p) {
            if (prdy < p + 1) prdy = wait_ge(C_PRDY, p + 1);
            CBAR();
            const float alpha = *(LAS float*)(L8 + ar + (p & (FD - 1)) * FSTR);
            v4u pf[2];
#pragma unroll
            for (int u = 0; u < 2; ++u) pf[u] = *(LAS v4u*)(L8 + pr + u * 512 + (p & (FD - 1)) * FSTR);
#pragma unroll
            for (int i = 0; i < 4; ++i) acc[i] *= alpha;
#pragma unroll
            for (int u = 0; u < 2; ++u) {
                unsigned vb[4];
#pragma unroll
                for (int i = 0; i < 4; ++i) { vb[i] = va[i] + slot * SLOT; PINV(vb[i]); PINV(acc[i]); }
                if (full < need + 2 * u) full = wait_ge(C_FULL, need + 2 * u);
                CBAR();
                v4u vf[4];
#pragma unroll
                for (int i = 0; i < 4; ++i) vf[i] = trfrag(L8 + vb[i], 4096);
                lds_st(ctl + (C_DONE0 + hf) * 4, 2 * p + u + 1);
                SB();
#pragma unroll
                for (int i = 0; i < 4; ++i) acc[i] = mfma(vf[i], pf[u], acc[i]);
                SB();
                slot = slot == NSLOT - 1 ? 0 : slot + 1;
            }
            need += p < NP - KP - 1 ? 8 : p == NP - KP - 1 ? 4 * KP + 4 : 4;
        }
        const bool up = c >= 8;
        const v4f u0 = up ? acc[2] : acc[0], u1 = up ? acc[3] : acc[1];
        st16(po, (v4u){clampu(pack2(u0[0], u0[1])), clampu(pack2(u0[2], u0[3])), clampu(pack2(u1[0], u1[1])), clampu(pack2(u1[2], u1[3]))});
        ST(4 + hf);
        DUMP(16 << hf);
    } else {
        const int w = wave;
        const int kpos = (lane & 32) | ((lane & 12) << 1) | ((lane & 16) >> 2) | (lane & 3);
        float xres;
        {
            if (w == 0) { ST(32); STV(0, t_entry); }
            BAR();
            asm volatile("s_waitcnt vmcnt(8)" : "+v"(xa), "+v"(xb), "+v"(xm), "+v"(xr), "+v"(epw)::"memory");
            xres = bflo(xr);
            float v[16];
#pragma unroll
            for (int j = 0; j < 4; ++j) { v[2 * j] = bflo(xa[j]); v[2 * j + 1] = bfhi(xa[j]); v[8 + 2 * j] = bflo(xb[j]); v[9 + 2 * j] = bfhi(xb[j]); }
            float s = 0.f;
#pragma unroll
            for (int j = 0; j < 16; ++j) s += v[j];
            const float mean = wave_sum(s) * (1.0f / 1024);
            float q = 0.f;
#pragma unroll
            for (int j = 0; j < 16; ++j) { const float d = v[j] - mean; q += d * d; }
            const float var = wave_sum(q) * (1.0f / 1024);
            const float sd = rbf(sqrtf(rbf(rbf(var) + 1e-5f)));
            *(LAS unsigned short*)(L8 + L_XS + (w * 64 + kpos) * 2) = f2bf(rbf(bflo(xm) - rbf(mean)) / sd);
            lds_inc(ctl + C_XS * 4);
        }
        const u64 tag = ((u64)epw[0] | ((u64)epw[1] << 32)) + 0x9E3779B97F4A7C15ull;
        const v2u tg = {(unsigned)tag, (unsigned)(tag >> 32)};
        const int trow = 4 * lg + (c >> 2);
        const unsigned ta128 = trow * 128 + 8 * (c & 3), tx128 = (trow >> 1) & 3;
        {
            wait_ge(C_XS, 4);
            v4u xf[8];
#pragma unroll
            for (int k = 0; k < 8; ++k) xf[k] = *(LAS v4u*)(L8 + L_XS + k * 64 + lg * 16);
            if (w == 0) ST(39);
            VM0();
            lds_inc(ctl + C_WQL * 4);
            wait_ge(C_WQL, 4);
            if (w == 0) ST(40);
            v4u wf[8];
#pragma unroll
            for (int k = 0; k < 8; ++k) wf[k] = trfrag(L8 + k * 4096 + ta128 + ((w ^ tx128) << 5), 2048);
            lds_inc(ctl + C_WQ * 4);
            v4f d0 = {0, 0, 0, 0}, d1 = {0, 0, 0, 0};
#pragma unroll
            for (int k = 0; k < 8; k += 2) { d0 = mfma(wf[k], xf[k], d0); d1 = mfma(wf[k + 1], xf[k + 1], d1); }
            d0 += d1;
            if (c < 2) st16(WS_QP + qr * 32768 + (qc * 64 + w * 16 + lg * 4 + 2 * c) * 8, (v4u){__float_as_uint(c ? d0[2] : d0[0]), __float_as_uint(c ? d0[3] : d0[1]), tg[0], tg[1]});
            if (w == 0) ST(8);
            nap<DLY_Q>();
            v4u q[8];
            const unsigned qo = WS_QP + g * 8192 + w * 2048 + lane * 16;
#pragma unroll
            for (int j = 0; j < 8; ++j) q[j] = j >= QEXP ? (v4u){0, 0, tg[0], tg[1]} : ld16(qo + (j >> 1) * 32768 + (j & 1) * 1024);
#pragma nounroll
            for (;;) {
                bool nd[8]; u64 again = 0;
#pragma unroll
                for (int j = 0; j < 8; ++j) { nd[j] = ((q[j][2] ^ tg[0]) | (q[j][3] ^ tg[1])) != 0; again |= __builtin_amdgcn_ballot_w64(nd[j]); }
                if (again == 0 || spent()) break;
                CBAR();
#pragma unroll
                for (int j = 0; j < 8; ++j) if (nd[j]) q[j] = ld16(qo + (j >> 1) * 32768 + (j & 1) * 1024);
            }
#pragma unroll
            for (int i = 0; i < 2; ++i) {
                const v4f s = (asf(q[i]) + asf(q[2 + i])) + (asf(q[4 + i]) + asf(q[6 + i]));
                *(LAS unsigned*)(L8 + L_QI + (2 * w + i) * 272 + lane * 4) = pack2(s[0], s[1]);
            }
            lds_inc(ctl + C_QRDY * 4);
        }
        v4u wof[8];
        {
            wait_ge(C_FULL, 12);
#pragma unroll
            for (int k = 0; k < 4; ++k)
#pragma unroll
                for (int t = 0; t < 2; ++t) wof[2 * k + t] = trfrag(L8 + 2 * SLOT + k * 8192 + trow * 256 + (((2 * w + t) ^ (trow & 7)) << 5) + 8 * (c & 3), 4096);
            lds_inc(ctl + C_WO * 4);
        }
        if (w == 3) {
            const v4u sent = {SENT, SENT, SENT, SENT};
#pragma unroll
            for (int i = 0; i < 2; ++i) st16(WS_ACC + ((unsigned)(g * 8 + 4 * i + lg) * 64 + (r & 63)) * 256 + c * 16, sent);
            if (lane < 4) st16(WS_ML + r * 128 + lane * 16, sent);
            if (lane < 32) st16(WS_P + head * 4096 + ocb * 512 + lane * 16, sent);
            if (ffn) {
                st16(WS_X + (cb * 4 + rb) * XREC + lane * 16, sent);
                if (lane < 2) st16(WS_X + (cb * 4 + rb) * XREC + 1024 + lane * 16, sent);
                st16(WS_Y + cb * 4096 + rb * 1024 + lane * 16, sent);
            }
            VM0();
            if (lane == 0) st8(WS_FLAG + r * 8, tg);
            ST(6);
            bool need = true;
#pragma nounroll
            for (;;) {
                nap<DLY_F>();
                if (need) {
                    const v4u f0 = ld16(WS_FLAG + lane * 16), f1 = ld16(WS_FLAG + 1024 + lane * 16);
                    need = (((f0[0] ^ tg[0]) | (f0[1] ^ tg[1]) | (f0[2] ^ tg[0])) | ((f0[3] ^ tg[1]) | (f1[0] ^ tg[0]) | (f1[1] ^ tg[1])) | ((f1[2] ^ tg[0]) | (f1[3] ^ tg[1]))) != 0;
                }
                bud -= 256;
                if (wnone(need) || spent()) break;
            }
            if (r == 0 && lane == 0) st8(WS_EPOCH, tg);
            lds_st(ctl + C_OK * 4, 1);
            ST(7);
            DUMP(0xC0);
        }
        if (DRY && w != 3) { nap<127>(); nap<127>(); }
#pragma nounroll
        for (int pass = DRY ? 0 : 1; pass < 2; ++pass) {
            int live = pass;
            asm volatile("" : "+s"(live));
            const u64 dry = live ? 0 : 1;
            const int lim = live ? 0 : -(1 << 20);
            rs = __builtin_amdgcn_make_buffer_rsrc(ws, 0, live ? WS_END : 0u, 0x00020000);
            if (live) {
#pragma nounroll
                while (lds_ld(ctl + C_KDONE * 4) < NT && !spent()) nap<8>();
                wait_ge(C_STOP, 1); wait_ge(C_DONE0, NT);
                if (w == 0) ST(21);
            }
            {
                nap<DLY_1>();
                v4u av[4];
                const unsigned ao = WS_ACC + (head * 64 + 16 * w + 4 * lg) * 256 + c * 16;
#pragma unroll
                for (int j = 0; j < 4; ++j) av[j] = ld16(ao + j * 256);
                wait_ge(C_WRDY, live);
                const v4f w4 = *(LAS v4f*)(L8 + L_W + (16 * w + 4 * lg) * 4);
                REPOLL(4, av, ao + j * 256);
                if (w == 0) ST(9);
                v2f e[4];
#pragma unroll
                for (int i = 0; i < 4; ++i) e[i] = (v2f){bflo(av[0][i]), bfhi(av[0][i])} * w4[0];
#pragma unroll
                for (int j = 1; j < 4; ++j)
#pragma unroll
                    for (int i = 0; i < 4; ++i) e[i] += (v2f){bflo(av[j][i]), bfhi(av[j][i])} * w4[j];
                *(LAS v4u*)(L8 + L_PART + (4 * w + lg) * 272 + c * 16) = (v4u){pack2(e[0][0], e[0][1]), pack2(e[1][0], e[1][1]), pack2(e[2][0], e[2][1]), pack2(e[3][0], e[3][1])};
                lds_inc(ctl + C_H1 * 4, live);
                v4u of[4];
#pragma nounroll
                for (;;) {
                    const int n = *(volatile LAS int*)(L8 + L_CTL + C_H1 * 4);
                    CBAR();
#pragma unroll
                    for (int k2 = 0; k2 < 4; ++k2) of[k2] = *(LAS v4u*)(L8 + L_PART + c * 272 + k2 * 64 + lg * 16);
                    SB();
                    if (rfl(n) >= 4 * live || spent()) break;
                }
                v4f d0 = {0, 0, 0, 0}, d1 = {0, 0, 0, 0};
#pragma unroll
                for (int k2 = 0; k2 < 4; ++k2) { d0 = mfma(of[k2], wof[2 * k2], d0); d1 = mfma(of[k2], wof[2 * k2 + 1], d1); }
                float p = swap16_add((d0[0] + d0[1]) + (d0[2] + d0[3]), (d1[0] + d1[1]) + (d1[2] + d1[3]));
                p = swap32_add(p, p);
                if (lane < 32) st4(WS_P + head * 4096 + ocb * 512 + w * 128 + lane * 4, clampw(p));
                if (w == 0) ST(10);
            }
            if (ffn) {
                {
                    v4u gf[8], uf[8];
                    auto frags = [&](int i) {
                        wait_ge(C_FULL, GKV + 4 * i + 4 + lim);
#pragma unroll
                        for (int k2 = 2 * i; k2 < 2 * i + 2; ++k2) {
                            LAS unsigned char* sp = L8 + i * SLOT + (k2 & 1) * 4096 + ta128 + ((w ^ tx128) << 5);
                            gf[k2] = trfrag(sp, 2048); uf[k2] = trfrag(sp + 8192, 2048);
                        }
                    };
                    nap<DLY_2>();
#pragma unroll
                    for (int i = 0; i < XPOLL / 4; ++i) frags(i);
                    if (XPOLL & 3) wait_ge(C_FULL, GKV + XPOLL + lim);
                    if (w == 0) ST(22);
                    v4u pv[8];
                    const unsigned po = WS_P + lg * 4096 + rb * 1024 + w * 256 + c * 16;
#pragma unroll
                    for (int j = 0; j < 8; ++j) pv[j] = ld16(po + j * 16384);
#pragma unroll
                    for (int i = XPOLL / 4; i < 4; ++i) frags(i);
                    REPOLL(8, pv, po + j * 16384);
                    if (w == 0) ST(11);
                    const v4f e = ((asf(pv[0]) + asf(pv[1])) + (asf(pv[2]) + asf(pv[3]))) + ((asf(pv[4]) + asf(pv[5])) + (asf(pv[6]) + asf(pv[7])));
                    const float h = rbf(xres + rbf(rows_reduce4(e)));
                    const int k = 4 * c + lg;
                    *(LAS unsigned short*)(L8 + L_HS + (w * 64 + ((k & 32) | ((k & 12) << 1) | ((k & 16) >> 2) | (k & 3))) * 2) = f2bf(h);
                    lds_inc(ctl + C_H2 * 4, live);
                    v4u hf[8];
#pragma nounroll
                    for (;;) {
                        const int n = *(volatile LAS int*)(L8 + L_CTL + C_H2 * 4);
                        CBAR();
#pragma unroll
                        for (int k2 = 0; k2 < 8; ++k2) hf[k2] = *(LAS v4u*)(L8 + ((c & 1) ? L_ONE : L_HS) + k2 * 64 + lg * 16);
                        SB();
                        if (rfl(n) >= 4 * live || spent()) break;
                    }
                    const float piv = __uint_as_float(rfl(__float_as_uint(h))), dd = h - piv;
                    const float s1 = wave_sum(dd), s2 = wave_sum(dd * dd);
                    if (lane == 0) st8(WS_X + (cb * 4 + rb) * XREC + 1024 + w * 8, (v2u){clampw(s1 + 64.f * piv), clampw(s2 - s1 * s1 * (1.0f / 64))});
                    v4f dg = {0, 0, 0, 0}, du = {0, 0, 0, 0};
#pragma unroll
                    for (int k2 = 0; k2 < 8; ++k2) { dg = mfma(hf[k2], gf[k2], dg); du = mfma(hf[k2], uf[k2], du); }
                    if (lane < 16) st16(WS_X + (cb * 4 + rb) * XREC + w * 256 + lane * 16, (v4u){clampw(dg[0]), clampw(du[0]), clampw(dg[1]), clampw(du[1])});
                    if (w == 0) ST(12);
                }
                {
                    nap<DLY_3>();
                    v4u xv[5];
                    const unsigned xo = WS_X + cb * 4 * XREC + lane * 16, so = WS_X + (cb * 4 + ((c >> 1) & 3)) * XREC + 1024 + (c & 1) * 16;
#pragma unroll
                    for (int j = 0; j < 5; ++j) xv[j] = ld16(j < 4 ? xo + j * XREC : so);
                    wait_ge(C_FULL, GKV + 16 + 2 * w + 2 + lim);
                    if (w == 0) ST(23);
                    v4u df[8];
#pragma unroll
                    for (int k2 = 0; k2 < 2; ++k2)
#pragma unroll
                        for (int t = 0; t < 4; ++t) df[4 * k2 + t] = trfrag(L8 + 4 * SLOT + w * 8192 + k2 * 4096 + ta128 + ((t ^ tx128) << 5), 2048);
                    REPOLL(5, xv, j < 4 ? xo + j * XREC : so);
                    if (w == 0) ST(24);
                    const v4f sv = asf(xv[4]);
                    float tot = sv[0] + sv[2];
                    tot += dpp<ROR4>(tot); tot += dpp<ROR2>(tot); tot += dpp<ROR1>(tot);
                    const float mean = tot * (1.0f / 1024);
                    const float dm0 = sv[0] * (1.0f / 64) - mean, dm1 = sv[2] * (1.0f / 64) - mean;
                    float m2 = (sv[1] + sv[3]) + 64.f * (dm0 * dm0 + dm1 * dm1);
                    m2 += dpp<ROR4>(m2); m2 += dpp<ROR2>(m2); m2 += dpp<ROR1>(m2);
                    const float mb = rbf(mean), isd = __builtin_amdgcn_rcpf(rbf(__builtin_amdgcn_sqrtf(rbf(rbf(m2 * (1.0f / 1024)) + 1e-5f))));
                    const v4f rr = (asf(xv[0]) + asf(xv[1])) + (asf(xv[2]) + asf(xv[3]));
                    const float gg = rbf((rr[0] - mb * rr[2]) * isd), uu = rbf((rr[1] - mb * rr[3]) * isd);
                    const float ex = __builtin_amdgcn_exp2f((-2.0f * 0.7978845608028654f * 1.4426950408889634f) * (gg + 0.044715f * gg * gg * gg));
                    *(LAS unsigned short*)(L8 + L_AS + w * 128 + kpos * 2) = f2bf(rbf(gg * __builtin_amdgcn_rcpf(1.0f + ex)) * uu);
                    CBAR();
                    const v4u a0 = *(LAS v4u*)(L8 + L_AS + w * 128 + lg * 16), a1 = *(LAS v4u*)(L8 + L_AS + w * 128 + 64 + lg * 16);
                    v4f d[4];
#pragma unroll
                    for (int t = 0; t < 4; ++t) { d[t] = mfma(a0, df[t], (v4f){0, 0, 0, 0}); d[t] = mfma(a1, df[4 + t], d[t]); }
                    const float y = (lg & 2) ? ((lg & 1) ? d[3][0] : d[2][0]) : ((lg & 1) ? d[1][0] : d[0][0]);
                    st4(WS_Y + cb * 4096 + rb * 1024 + w * 256 + lane * 4, clampw(y));
                    if (w == 0) ST(13);
                }
            } else if (w == 0) {
                const __amdgpu_buffer_rsrc_t ro = __builtin_amdgcn_make_buffer_rsrc(out, 0, live ? 2048u : 0u, 0x00020000);
                const unsigned fo = (lane >> 3) * 4096 + fa * 128 + (lane & 7) * 16;
                nap<DLY_2 + 16>();
                v4u pv[4];
#pragma unroll
                for (int j = 0; j < 4; ++j) pv[j] = ld16(WS_P + fo + j * 32768);
                REPOLL(4, pv, WS_P + fo + j * 32768);
                float hs = rows_reduce4((asf(pv[0]) + asf(pv[1])) + (asf(pv[2]) + asf(pv[3])));
                hs += dpp<ROR8>(hs);
                const float h = rbf(xres + rbf(hs));
                ST(14);
                nap<DLY_4>();
                v4u yv[7];
#pragma unroll
                for (int j = 0; j < 7; ++j) yv[j] = ld16(WS_Y + fo + j * 32768);
                REPOLL(7, yv, WS_Y + fo + j * 32768);
                float ys = rows_reduce4(((asf(yv[0]) + asf(yv[1])) + (asf(yv[2]) + asf(yv[3]))) + ((asf(yv[4]) + asf(yv[5])) + asf(yv[6])));
                ys += dpp<ROR8>(ys);
                if (c < 8) __builtin_amdgcn_raw_buffer_store_b16(f2bf(h + rbf(ys)), ro, fcol * 2, 0, 0);
                ST(15);
            }
        }
        if (w == 0) DUMP(ffn ? 0x18101E03F01ull : 0x1810020C701ull);
        if (w == 0 && (PINF == 0 || (PINF == 1) == !ffn)) {
            LGKM0();
            u64 tp = *(volatile LAS u64*)(L8 + L_STAMP + (PIN & 63) * 8);
            if (PIN == 64) tp = __hip_atomic_load((GAS u64*)(ws + WS_STAMP), RLX_AGENT);
            long long add = (long long)(tp - t_entry);
            add = add > 20000 ? 20000 : add < -3000 ? -3000 : add;
            const u64 until = t_entry + 6000 + add;
            for (int i = 0; i < 1000000 && __builtin_amdgcn_s_memrealtime() < until; ++i) __builtin_amdgcn_s_sleep(1);
        }
    }
}

extern "C" void kernel_launch(void* const* d_in, const int* in_sizes, int n_in, void* d_out, int out_size, void* d_ws, size_t ws_size, hipStream_t stream) {
    (void)in_sizes; (void)n_in; (void)out_size; (void)ws_size;
    (void)hipFuncSetAttribute((const void*)fused, hipFuncAttributeMaxDynamicSharedMemorySize, L_END);
    fused<<<NWG, 512, L_END, stream>>>((const unsigned short*)d_in[0], (const unsigned short*)d_in[3], (unsigned char*)d_ws, (const unsigned short*)d_in[1],
                                       (const unsigned short*)d_in[2], (const unsigned short*)d_in[4], (const unsigned short*)d_in[6], (const unsigned short*)d_in[7],
                                       (const unsigned short*)d_in[8], (unsigned short*)d_out);
}
```

```cpp
#define XPOLL 8
#define DLY_2 16
#define HOLD1 35
#define HOLD2 38
#define W_G 32
#define W_U 32
#define W_D 36
#include <hip/hip_runtime.h>
#include <stdint.h>

typedef unsigned v4u __attribute__((ext_vector_type(4)));
typedef unsigned v2u __attribute__((ext_vector_type(2)));
typedef float v4f __attribute__((ext_vector_type(4)));
typedef float v2f __attribute__((ext_vector_type(2)));
typedef __bf16 v8bf __attribute__((ext_vector_type(8)));
typedef __bf16 v2bf __attribute__((ext_vector_type(2)));
typedef short v4s __attribute__((ext_vector_type(4)));
typedef unsigned v8u __attribute__((ext_vector_type(8)));
typedef __bf16 v16bf __attribute__((ext_vector_type(16)));
typedef unsigned long long u64;
#define GAS __attribute__((address_space(1)))
#define LAS __attribute__((address_space(3)))
#define RLX_AGENT __ATOMIC_RELAXED, __HIP_MEMORY_SCOPE_AGENT

#define W_MAIN 28
#define W_MIN 20
#define W_MAX 36
#define W_O 24
#define W_TLO 0
#define W_THI 0
#define HEADB 2
#define DLY_F 20
#define DLY_Q 40
#define DLY_ML 70
#define DLY_MP 20
#define DLY_5 20
#define DLY_1 8
#define DLY_3 12
#define DLY_4 50
#define PACE_A 163
#define PACE_B 886
#define PACE_K 20
#define PF 1
#define TOUCH 0
#define HOLD 0
#define BARPOS 0
#define MLB 24
#define T1 520
#define T2 0
#define T3 0
#define T4 0
#define SHARE 0

constexpr int NWG = 256, SLOT = 16384;
constexpr unsigned XREC = 1152;
constexpr unsigned WS_EPOCH = 0, WS_QP = 4096, WS_ACC = WS_QP + 4 * 4096 * 8, WS_ML = WS_ACC + 32 * 64 * 256,
                   WS_P = WS_ML + 256 * 128, WS_X = WS_P + 32 * 4096, WS_Y = WS_X + 224 * XREC, WS_END = WS_Y + 56 * 4096, WS_STAMP = 4u << 20;
constexpr int L_PF = 8 * SLOT, L_QI = L_PF + 16384, L_XS = L_QI + 8 * 272, L_PART = L_XS + 512, L_HS = L_PART + 16 * 272, L_ONE = L_HS + 2 * 272, L_W = L_ONE + 256, L_AS = L_W + 256,
              L_MX = L_AS + 512, L_CTL = L_MX + 256, L_STAMP = L_CTL + 256, L_END = L_STAMP + 512;
enum { C_NX = 0, C_WQ = 3, C_FULL, C_MX, C_PRDY, C_XS, C_WQL, C_QRDY, C_OK, C_WRDY, C_H1, C_H2, C_GATE, C_TAG = 16 };
static_assert(W_MIN >= 16 && W_MAX <= 36 && SLOT == 0x4000, "window");
static_assert(L_END <= 160 * 1024 && (L_XS & 15) == 0 && (L_CTL & 15) == 0, "lds");
constexpr unsigned SENT = 0xFFFFFFFFu;
constexpr int SPIN = 1 << 22;

__device__ __forceinline__ float bflo(unsigned u) { return __uint_as_float(u << 16); }
__device__ __forceinline__ float bfhi(unsigned u) { return __uint_as_float(u & 0xffff0000u); }
__device__ __forceinline__ unsigned short f2bf(float f) { __bf16 b = (__bf16)f; return __builtin_bit_cast(unsigned short, b); }
__device__ __forceinline__ unsigned pack2(float a, float b) { return __builtin_bit_cast(unsigned, __builtin_convertvector((v2f){a, b}, v2bf)); }
__device__ __forceinline__ float rbf(float f) { return __uint_as_float(pack2(0.f, f)); }
__device__ __forceinline__ unsigned clampu(unsigned u) { return u < 0xFFFFFFFEu ? u : 0xFFFFFFFEu; }
__device__ __forceinline__ unsigned umax4(v4u v) { return max(max(v[0], v[1]), max(v[2], v[3])); }
__device__ __forceinline__ v4f asf(v4u v) { return __builtin_bit_cast(v4f, v); }

template <int CTRL> __device__ __forceinline__ float dpp(float v) {
    return __builtin_bit_cast(float, __builtin_amdgcn_update_dpp(0, __builtin_bit_cast(int, v), CTRL, 0xf, 0xf, true));
}
constexpr int ROR1 = 0x121, ROR2 = 0x122, ROR4 = 0x124, ROR8 = 0x128;
__device__ __forceinline__ float row_sum(float v) { v += dpp<ROR8>(v); v += dpp<ROR4>(v); v += dpp<ROR2>(v); v += dpp<ROR1>(v); return v; }
__device__ __forceinline__ float row_max(float v) { v = fmaxf(v, dpp<ROR8>(v)); v = fmaxf(v, dpp<ROR4>(v)); v = fmaxf(v, dpp<ROR2>(v)); v = fmaxf(v, dpp<ROR1>(v)); return v; }
__device__ __forceinline__ float swap16_add(float a, float b) {
    const auto r = __builtin_amdgcn_permlane16_swap(__float_as_uint(a), __float_as_uint(b), false, false);
    return __uint_as_float(r[0]) + __uint_as_float(r[1]);
}
__device__ __forceinline__ float swap32_add(float a, float b) {
    const auto r = __builtin_amdgcn_permlane32_swap(__float_as_uint(a), __float_as_uint(b), false, false);
    return __uint_as_float(r[0]) + __uint_as_float(r[1]);
}
__device__ __forceinline__ float x16_max(float a) {
    const auto r = __builtin_amdgcn_permlane16_swap(__float_as_uint(a), __float_as_uint(a), false, false);
    return fmaxf(__uint_as_float(r[0]), __uint_as_float(r[1]));
}
__device__ __forceinline__ float x32_max(float a) {
    const auto r = __builtin_amdgcn_permlane32_swap(__float_as_uint(a), __float_as_uint(a), false, false);
    return fmaxf(__uint_as_float(r[0]), __uint_as_float(r[1]));
}
__device__ __forceinline__ float wave_sum(float v) { v = row_sum(v); v = swap16_add(v, v); return swap32_add(v, v); }
__device__ __forceinline__ float wave_max(float v) { return x32_max(x16_max(row_max(v))); }
__device__ __forceinline__ float rows_reduce4(v4f v) { return swap32_add(swap16_add(v[0], v[1]), swap16_add(v[2], v[3])); }

__device__ __forceinline__ v4f mfma(v4u a, v4u b, v4f c) {
    return __builtin_amdgcn_mfma_f32_16x16x32_bf16(__builtin_bit_cast(v8bf, a), __builtin_bit_cast(v8bf, b), c, 0, 0, 0);
}
__device__ __forceinline__ v4f smf(v4u a, v8u b, v4f c, int idx) {
    return __builtin_amdgcn_smfmac_f32_16x16x64_bf16(__builtin_bit_cast(v8bf, a), __builtin_bit_cast(v16bf, b), c, idx, 0, 0);
}
__device__ __forceinline__ v2u trread(LAS unsigned char* p) {
    return __builtin_bit_cast(v2u, __builtin_amdgcn_ds_read_tr16_b64_v4i16((LAS v4s*)p));
}
__device__ __forceinline__ v4u trfrag(LAS unsigned char* p, int hi_off) { const v2u a = trread(p), b = trread(p + hi_off); return (v4u){a[0], a[1], b[0], b[1]}; }

__device__ __forceinline__ v8u trfrag2(LAS unsigned char* p, LAS unsigned char* q, int hi_off) {
    const v2u a = trread(p), b = trread(p + hi_off), c = trread(q), d = trread(q + hi_off);
    return (v8u){a[0], a[1], b[0], b[1], c[0], c[1], d[0], d[1]};
}
__device__ __forceinline__ unsigned bfi(unsigned m, unsigned t, float a) { return (m & t) | (~m & __float_as_uint(a)); }
__device__ __forceinline__ v4u tagged(float a, float b, float c, float d, v4u t, unsigned m) { return (v4u){bfi(m, t[0], a), bfi(m, t[1], b), bfi(m, t[2], c), bfi(m, t[3], d)}; }
__device__ __forceinline__ unsigned xo(unsigned a, unsigned b, unsigned c) { return __builtin_amdgcn_bitop3_b32(a, b, c, 0xBE); }
__device__ __forceinline__ unsigned or4(v4u v, unsigned r) { return (v[0] | v[1] | r) | v[2] | v[3]; }

#define CBAR() asm volatile("" ::: "memory")
#define LGKM0() asm volatile("s_waitcnt lgkmcnt(0)" ::: "memory")
#define VM0() asm volatile("s_waitcnt vmcnt(0)" ::: "memory")
#define BAR() asm volatile("s_barrier" ::: "memory")
#define SB() __builtin_amdgcn_sched_barrier(0)
#define PINV(x) asm volatile("" : "+v"(x))
template <int N> __device__ __forceinline__ void vmwait() { asm volatile("s_waitcnt vmcnt(%0)" ::"n"(N) : "memory"); }
template <int N> __device__ __forceinline__ void nap() { if constexpr (N > 0) asm volatile("s_sleep %0" ::"n"(N) : "memory"); }

__device__ __forceinline__ void dma4(const void* sbase, unsigned dst, unsigned v0, unsigned v1, unsigned v2, unsigned v3) {
    unsigned keep;
    asm volatile("s_mov_b32 %0, m0\n\ts_mov_b32 m0, %2\n\ts_nop 0\n\t"
                 "global_load_lds_dwordx4 %3, %1 nt\n\tglobal_load_lds_dwordx4 %4, %1 offset:1024 nt\n\t"
                 "global_load_lds_dwordx4 %5, %1 offset:2048 nt\n\tglobal_load_lds_dwordx4 %6, %1 offset:3072 nt\n\ts_mov_b32 m0, %0"
                 : "=&s"(keep) : "s"(sbase), "s"(dst), "v"(v0), "v"(v1), "v"(v2), "v"(v3) : "memory");
}
__device__ __forceinline__ int lds_ld(unsigned addr) {
    unsigned v;
    asm volatile("ds_read_b32 %0, %1\n\ts_waitcnt lgkmcnt(0)" : "=&v"(v) : "v"(addr) : "memory");
    return __builtin_amdgcn_readfirstlane(v);
}
__device__ __forceinline__ v4u lds_ld4(unsigned addr) {
    v4u v;
    asm volatile("ds_read_b128 %0, %1\n\ts_waitcnt lgkmcnt(0)" : "=&v"(v) : "v"(addr) : "memory");
    return v;
}
__device__ __forceinline__ void lds_st(unsigned addr, int v) { asm volatile("ds_write_b32 %0, %1" ::"v"(addr), "v"(v) : "memory"); }
__device__ __forceinline__ void lds_inc(unsigned addr, int n = 1) { asm volatile("s_mov_b64 exec, 1\n\tds_add_u32 %0, %1\n\ts_mov_b64 exec, -1" ::"v"(addr), "v"(n) : "memory"); }
__device__ __forceinline__ float max3(float a, float b, float c) { float d; asm("v_max3_f32 %0, %1, %2, %3" : "=v"(d) : "v"(a), "v"(b), "v"(c)); return d; }
__device__ __forceinline__ float max_over8(float a) {
    float t;
    asm("s_nop 1\n\tv_max_f32_dpp %0, %0, %0 row_ror:8 row_mask:0xf bank_mask:0xf\n\tv_mov_b32 %1, %0\n\ts_nop 1\n\tv_permlane16_swap_b32 %0, %1\n\ts_nop 0\n\tv_max_f32 %0, %0, %1\n\t"
        "v_mov_b32 %1, %0\n\ts_nop 1\n\tv_permlane32_swap_b32 %0, %1\n\ts_nop 0\n\tv_max_f32 %0, %0, %1" : "+v"(a), "=&v"(t));
    return a;
}
__device__ __forceinline__ int rfl(unsigned v) { return __builtin_amdgcn_readfirstlane(v); }

__device__ __forceinline__ bool wnone(bool p) { return __builtin_amdgcn_ballot_w64(p) == 0ull; }

#define ST(i) do {} while (0)
#define STV(i, v) do {} while (0)
#define DUMP(mask) do {} while (0)

__global__ void __launch_bounds__(512, 2) fused(const unsigned short* __restrict__ x, const unsigned short* __restrict__ wq, unsigned char* __restrict__ ws,
                                                const unsigned short* __restrict__ Kc, const unsigned short* __restrict__ Vc, const unsigned short* __restrict__ wo,
                                                const unsigned short* __restrict__ wg, const unsigned short* __restrict__ wu, const unsigned short* __restrict__ wd,
                                                unsigned short* __restrict__ out) {
    const u64 t_entry = __builtin_amdgcn_s_memrealtime();
    extern __shared__ __attribute__((aligned(16))) unsigned char smem[];
    LAS unsigned char* L8 = (LAS unsigned char*)smem;
    const unsigned lbase = (unsigned)(uintptr_t)L8;
    const int lane = threadIdx.x & 63, wave = __builtin_amdgcn_readfirstlane(threadIdx.x >> 6), b = blockIdx.x;
    const int c = lane & 15, lg = lane >> 4;
    u64 stm = 0; (void)stm;
    if (PF) {
        u64 pc; unsigned len, keep, o = (((b >> 3) & 15) * 64 + lane) * 32;
        asm volatile("s_cmp_lg_i32 %[wv], 7\n\ts_cbranch_scc1 .Lskpf%=\n\ts_getpc_b64 %[pc]\n.Lpc%=:\n\ts_mov_b32 %[len], .Lend%=-4-.Lpc%=\n\ts_max_i32 %[len], %[len], 0\n\tv_min_u32 %[o], %[len], %[o]\n\t"
                     "s_mov_b32 %[keep], m0\n\ts_mov_b32 m0, %[d]\n\ts_nop 0\n\tglobal_load_lds_dword %[o], %[pc]\n\ts_mov_b32 m0, %[keep]\n.Lskpf%=:\n\t.subsection 1\n.Lend%=:\n\t.subsection 0"
                     : [pc] "=&s"(pc), [len] "=&s"(len), [keep] "=&s"(keep), [o] "+v"(o) : [wv] "s"(wave), [d] "s"(lbase + L_W) : "memory", "scc");
    }
    *(LAS unsigned*)(L8 + L_CTL + lane * 4) = 0u;
    *(LAS unsigned*)(L8 + L_ONE + lane * 4) = 0x3F803F80u;
    const int xc = b & 7, jj = b >> 3, r = xc * 32 + jj;
    const int g = r >> 6, head = r >> 3, ocb = r & 7;
    const int qr = (r >> 4) & 3, qc = g * 16 + (r & 15);
    const bool ffn = jj < 28;
    const int rb = xc >> 1, cb = (xc & 1) * 28 + jj;
    const unsigned ctl = lbase + L_CTL;
    const unsigned r8 = lane >> 3, ch8 = ((lane & 7) ^ (((r8 >> 1) & 3) << 1)) << 4;
    const int fa = xc * 4 + jj - 28, fcol = (fa >> 1) * 64 + 16 * lg + 8 * (fa & 1) + (lane & 7);
    v4u xa, xb; v2u epw; unsigned xm, xr;
    {
        const unsigned v = (qr * 256 + (wave & 3) * 64 + r8) * 8192 + qc * 128 + ch8, d = 8 * 8192 - 1024, u = v + 32 * 8192;
        unsigned keep;
        asm volatile("s_cmp_gt_i32 %[wv], 3\n\ts_cbranch_scc1 .Lskip%=\n\t"
                     "global_load_dwordx4 %[xa], %[o0], %[x]\n\tglobal_load_dwordx4 %[xb], %[o0], %[x] offset:16\n\t"
                     "global_load_ushort %[xm], %[o1], %[x]\n\tglobal_load_ushort %[xr], %[o2], %[x]\n\tglobal_load_dwordx2 %[ep], %[z], %[ws] sc1\n\t"
                     "s_mov_b32 %[keep], m0\n\ts_mov_b32 m0, %[d0]\n\ts_nop 0\n\t"
                     "global_load_lds_dwordx4 %[v0], %[b0] nt\n\tglobal_load_lds_dwordx4 %[v1], %[b0] offset:1024 nt\n\t"
                     "global_load_lds_dwordx4 %[v2], %[b0] offset:2048 nt\n\tglobal_load_lds_dwordx4 %[v3], %[b0] offset:3072 nt\n\t"
                     "s_mov_b32 m0, %[d1]\n\ts_nop 0\n\t"
                     "global_load_lds_dwordx4 %[u0], %[b0] nt\n\tglobal_load_lds_dwordx4 %[u1], %[b0] offset:1024 nt\n\t"
                     "global_load_lds_dwordx4 %[u2], %[b0] offset:2048 nt\n\tglobal_load_lds_dwordx4 %[u3], %[b0] offset:3072 nt\n\t"
                     "s_mov_b32 m0, %[keep]\n.Lskip%=:"
                     : [xa] "=&v"(xa), [xb] "=&v"(xb), [xm] "=&v"(xm), [xr] "=&v"(xr), [ep] "=&v"(epw), [keep] "=&s"(keep)
                     : [wv] "s"(wave), [x] "s"(x), [ws] "s"(ws), [z] "v"(WS_EPOCH), [o0] "v"(lane * 32), [o1] "v"((qr * 256 + (wave & 3) * 64 + lane) * 2),
                       [o2] "v"((ffn ? rb * 256 + (wave & 3) * 64 + lane : fcol) * 2), [b0] "s"(wq), [d0] "s"(lbase + 6 * SLOT + wave * 8192), [d1] "s"(lbase + 6 * SLOT + wave * 8192 + 4096),
                       [v0] "v"(v), [v1] "v"(v + d), [v2] "v"(v + 2 * d), [v3] "v"(v + 3 * d), [u0] "v"(u), [u1] "v"(u + d), [u2] "v"(u + 2 * d), [u3] "v"(u + 3 * d)
                     : "memory", "scc");
    }
    const unsigned kvo = SHARE ? ((r & ~63) * 16 + ((r & 63) >> 1) * 16 + (r & 1)) * SLOT : r * 16 * SLOT;
    unsigned a0 = kvo + lg * 256 + ((c ^ lg) << 4), a1 = a0 ^ 64, a2 = a0 ^ 128, a3 = a0 ^ 192;
#define LD4 "global_load_lds_dwordx4 %[a0], %[b] nt\n\tglobal_load_lds_dwordx4 %[a1], %[b] offset:1024 nt\n\t" \
            "global_load_lds_dwordx4 %[a2], %[b] offset:2048 nt\n\tglobal_load_lds_dwordx4 %[a3], %[b] offset:3072 nt\n\t"
#define ADV(S) "v_add_u32 %[a0], " S ", %[a0]\n\tv_add_u32 %[a1], " S ", %[a1]\n\tv_add_u32 %[a2], " S ", %[a2]\n\tv_add_u32 %[a3], " S ", %[a3]\n\t"
    asm volatile("s_waitcnt lgkmcnt(0)\n\ts_barrier" ::"s"(t_entry), "s"(wg), "s"(wd) : "memory");
    {
        unsigned keep, t;
        asm volatile("s_cmp_lg_i32 %[wv], 4\n\ts_cbranch_scc1 .Lskiq%=\n\ts_mov_b32 %[keep], m0\n\ts_mov_b32 m0, %[lb]\n\ts_mov_b32 %[t], 0\n"
                     ".Lhd%=:\n\t" LD4 "s_add_i32 m0, m0, 0x1000\n\ts_add_i32 %[t], %[t], 1\n\t" ADV("0x1000")
                     "s_cmp_lt_u32 m0, %[le]\n\ts_cbranch_scc1 .Lhd%=\n\t"
                     "s_mov_b32 m0, %[keep]\n.Lskiq%=:"
                     : [keep] "=&s"(keep), [t] "=&s"(t), [a0] "+v"(a0), [a1] "+v"(a1), [a2] "+v"(a2), [a3] "+v"(a3)
                     : [wv] "s"(wave), [lb] "s"(lbase), [le] "s"(lbase + HEADB * SLOT), [b] "s"(Kc) : "memory", "scc");
    }
    const __amdgpu_buffer_rsrc_t rs = __builtin_amdgcn_make_buffer_rsrc(ws, 0, WS_END, 0x00020000);
    const float LOG2E = 1.4426950408889634f;
    int bud = SPIN;
    auto wait_ge = [&](int word, int need) -> int {
        int v; unsigned t;
        bud = __builtin_amdgcn_readfirstlane(bud); need = __builtin_amdgcn_readfirstlane(need);
        asm volatile(".Lw%=:\n\tds_read_b32 %1, %3\n\ts_waitcnt lgkmcnt(0)\n\tv_readfirstlane_b32 %0, %1\n\ts_cmp_ge_i32 %0, %4\n\ts_cbranch_scc1 .Ld%=\n\t"
                     "s_sub_i32 %2, %2, 1\n\ts_cmp_ge_i32 %2, 0\n\ts_cbranch_scc1 .Lw%=\n.Ld%=:"
                     : "=&s"(v), "=&v"(t), "+s"(bud) : "v"(ctl + word * 4), "s"(need) : "memory", "scc");
        return v;
    };
    auto spent = [&]() -> bool { bud -= 8; return bud < 0; };
    auto ld16 = [&](unsigned off) -> v4u { return __builtin_amdgcn_raw_buffer_load_b128(rs, off, 0, 16); };
    auto st16 = [&](unsigned off, v4u v) { __builtin_amdgcn_raw_buffer_store_b128(v, rs, off, 0, 16); };
    auto st8 = [&](unsigned off, v2u v) { __builtin_amdgcn_raw_buffer_store_b64(v, rs, off, 0, 16); };
#define TAGPOLL(N, V, OFF, EXTRA, EXTRAOFF) do { _Pragma("nounroll") for (;;) { unsigned st = 0; \
                _Pragma("unroll") for (int j = 0; j < N; ++j) { V[j] ^= t4; st = or4(V[j], st); } \
                const bool ex = EXTRA && xo(V[N - 1 + EXTRA][2], tg[0], V[N - 1 + EXTRA][3] ^ tg[1]) != 0; \
                if (wnone((st & 0xFFFu) != 0 || ex)) break; \
                if (spent()) break; \
                CBAR(); \
                _Pragma("unroll") for (int j = 0; j < N; ++j) { PINV(V[j]); const bool nd = or4(V[j], 0) & 0xFFFu; V[j] ^= t4; PINV(V[j]); if (nd) V[j] = ld16(OFF); } \
                if (ex) V[N - 1 + EXTRA] = ld16(EXTRAOFF); } } while (0)
#define REPOLL(N, V, OFF) do { _Pragma("nounroll") for (;;) { bool nd[N]; u64 again = 0; \
                _Pragma("unroll") for (int j = 0; j < N; ++j) { nd[j] = umax4(V[j]) == SENT; again |= __builtin_amdgcn_ballot_w64(nd[j]); } \
                if (again == 0) break; \
                if (spent()) break; \
                CBAR(); \
                _Pragma("unroll") for (int j = 0; j < N; ++j) if (nd[j]) V[j] = ld16(OFF); } } while (0)

    const int trow = 4 * lg + (c >> 2);
    if (wave == 4) {
        ST(38);
        int n = HEADB;
        int pub = 0;
        auto publish = [&](int v) { pub = max(pub, v); lds_st(ctl + C_FULL * 4, pub); };
        auto drain = [&]() {
            const int gi = 4 * n;
            vmwait<32>(); publish(gi - 8); vmwait<28>(); publish(gi - 7); vmwait<24>(); publish(gi - 6); vmwait<20>(); publish(gi - 5); vmwait<16>(); publish(gi - 4);
            vmwait<12>(); publish(gi - 3); vmwait<8>(); publish(gi - 2); vmwait<4>(); publish(gi - 1); vmwait<0>(); publish(gi);
        };
        const unsigned vv0 = lg * 256 + ((c ^ (lg << 1)) << 4), vv1 = vv0 ^ 128;
        const unsigned o0 = vv0 + lg * 1792 + head * 128 * 2048 + ocb * 256, o1 = (o0 ^ 128) + 7168, e0 = (rb * 256 + r8) * 7168 + cb * 128 + ch8, f0 = (cb * 64 + r8) * 2048 + rb * 512 + ch8;
        unsigned nx[5][4] = {{kvo + vv0, kvo + vv1, kvo + vv0, kvo + vv1}, {o0, o1, o0 + 14336, o1 + 14336}, {e0, e0 + 56320, e0 + 2 * 56320, e0 + 3 * 56320},
                             {e0, e0 + 56320, e0 + 2 * 56320, e0 + 3 * 56320}, {f0, f0 + 15360, f0 + 2 * 15360, f0 + 3 * 15360}};
        const unsigned short* bs = Kc;
        const unsigned short* nb[5] = {Vc, wo, wg, wu, wd};
        u64 gtab = 32ull | 256ull << 12 | 1792ull << 24 | 1792ull << 36 | 1ull << 48;
        u64 wtab = (u64)(W_MAIN - 4) | (u64)(W_O - 4) << 8 | (u64)(W_G - 4) << 16 | (u64)(W_U - 4) << 24 | (u64)(W_D - 4) << 32;
        int gs = 4096, ga = 4096, wb = W_MAIN - 4, wlo = W_MIN - 4, whi = W_MAX - 4, seen = -2, lim = 16;
        const int last = ffn ? 40 : 34;
        int tref = PACE_A * 16 + HEADB * PACE_B;
        unsigned now = (unsigned)__builtin_amdgcn_s_memrealtime();
        int nst = 1, lst = 0;
#define LINK(N, L) "s_cmp_ge_i32 %[nw], " #N "\n\ts_waitcnt vmcnt(" #N ")\n\ts_cbranch_scc1 ." #L "%=\n\t"
#define WAITV(L) LINK(32, L) LINK(28, L) LINK(24, L) LINK(20, L) LINK(16, L) "s_waitcnt vmcnt(12)\n." #L "%=:\n\t"
#define G4V(L) WAITV(L) LD4 "ds_write_b32 %[pa], %[pv]\n\tv_add_u32 %[pv], 1, %[pv]\n\ts_add_i32 m0, m0, 0x1000\n\t" ADV("%[gs]")
#pragma nounroll
        for (;;) {
#pragma nounroll
            do {
                int nw = wb + (((int)(now - (unsigned)t_entry) - (tref >> 4)) * PACE_K >> 8);
                asm("s_max_i32 %0, %0, %1\n\ts_min_i32 %0, %0, %2\n\ts_and_b32 %0, %0, -4" : "+s"(nw) : "s"(wlo), "s"(whi) : "scc");
                tref += PACE_B;
                if (seen < n - 7) {
#pragma nounroll
                    for (int again = 0;; again = 1) {
                        const v4u cw = lds_ld4(ctl);
                        seen = min(rfl(min(min(cw[0], cw[1]), cw[2])), rfl(cw[3] < 4 ? -2 : cw[3] < 8 ? HOLD1 - 8 : cw[3] < 12 ? HOLD2 - 8 : 64));
                        if (seen >= n - 7 || spent()) break;
                        if (!again) { ++nst; lst = n; drain(); }
                    }
                }
                if (SHARE) ga = n >= 32 ? ga : (n & 15) == 7 ? 4096 + 497 * SLOT : 4096 + SLOT;
                unsigned keep, pv = 4 * n - (nw >> 2); u64 tm;
                asm volatile("s_mov_b32 %[keep], m0\n\ts_mov_b32 m0, %[d]\n\t" G4V(La) G4V(Lb) "s_memrealtime %[now]\n\t" G4V(Lc) WAITV(Ld) "s_waitcnt lgkmcnt(0)\n\t" LD4
                             "ds_write_b32 %[pa], %[pv]\n\ts_mov_b32 m0, %[keep]\n\t" ADV("%[ga]")
                             : [keep] "=&s"(keep), [now] "=&s"(tm), [pv] "+v"(pv), [a0] "+v"(a0), [a1] "+v"(a1), [a2] "+v"(a2), [a3] "+v"(a3)
                             : [nw] "s"(nw), [b] "s"(bs), [d] "s"(lbase + (n & 7) * SLOT), [pa] "v"(ctl + C_FULL * 4), [gs] "s"(gs), [ga] "s"(ga) : "memory", "scc");
                now = rfl((unsigned)tm);
                ++n;
            } while (n < lim);
            if (n == last) break;
            lim = max(n + 2, 32);
            gs = ((unsigned)gtab & 0xFFF) * 128; ga = gs == 128 ? 32 * 2048 - 384 : gs; gtab >>= 12;
            wb = (unsigned)wtab & 0xFF; wtab >>= 8;
            if (n >= 32) { wlo = wb - W_TLO; whi = wb + W_THI; }
            a0 = nx[0][0]; a1 = nx[0][1]; a2 = nx[0][2]; a3 = nx[0][3]; bs = nb[0];
#pragma unroll
            for (int i = 0; i < 4; ++i) {
                nb[i] = nb[i + 1];
#pragma unroll
                for (int j = 0; j < 4; ++j) nx[i][j] = nx[i + 1][j];
            }
        }
        if (ffn) ST(54);
        STV(29, nst); STV(30, lst + 100);
        drain();
        ST(ffn ? 2 : 45);
    } else if (wave > 4) {
        const int j3 = wave - 5;
        v4u qlo[4], qhi[4];
        unsigned ka[4];
#pragma unroll
        for (int i = 0; i < 4; ++i) ka[i] = c * 256 + (((4 * i + lg) ^ c) << 4);
        const unsigned pw = L_PF + (c & 7) * 64 + lg * 16 + (c >> 3) * 8;
        float m = -INFINITY, l = 0.f;
        const float scale = 0.08838834764831845f;
        int full = 0;
        VM0();
        nap<40>();
#pragma nounroll
        for (int p = j3; p < 16; p += 3) {
            LAS unsigned char* sp = L8 + (p & 7) * SLOT;
            if (full < 4 * p + 2) full = wait_ge(C_FULL, 4 * p + 2);
            CBAR();
            v4u kf[16];
#pragma unroll
            for (int i = 0; i < 4; ++i) { kf[i] = *(LAS v4u*)(sp + ka[i]); kf[4 + i] = *(LAS v4u*)(sp + ka[i] + 4096); }
            if (full < 4 * p + 4) full = wait_ge(C_FULL, 4 * p + 4);
            CBAR();
#pragma unroll
            for (int i = 0; i < 4; ++i) { kf[8 + i] = *(LAS v4u*)(sp + ka[i] + 8192); kf[12 + i] = *(LAS v4u*)(sp + ka[i] + 12288); }
            lds_st(ctl + (C_NX + j3) * 4, p == 15 ? 64 : min(p + 3, 16));
            if (p < 3) {
                wait_ge(C_QRDY, 4);
#pragma unroll
                for (int i = 0; i < 4; ++i) {
                    const v4u f = *(LAS v4u*)(L8 + L_QI + (c & 7) * 272 + (4 * i + lg) * 16), z = {0, 0, 0, 0};
                    qlo[i] = c < 8 ? f : z; qhi[i] = c < 8 ? z : f;
                }
                if (p == 0) ST(3);
            }
            SB();
            v4f s0 = {0, 0, 0, 0}, s1 = {0, 0, 0, 0};
#pragma unroll
            for (int i = 0; i < 4; ++i) { s0 = mfma(kf[i], qlo[i], s0); s1 = mfma(kf[8 + i], qlo[i], s1); s0 = mfma(kf[4 + i], qhi[i], s0); s1 = mfma(kf[12 + i], qhi[i], s1); }
            unsigned e[4];
#pragma unroll
            for (int i = 0; i < 4; ++i) {
                const v4f sv = i < 2 ? s0 : s1;
                const v2f t = (v2f){rbf(sv[2 * (i & 1)]), rbf(sv[2 * (i & 1) + 1])} * scale;
                e[i] = pack2(t[0], t[1]);
                m = max3(m, t[0], t[1]);
            }
            *(LAS v2u*)(L8 + pw + p * 1024) = (v2u){e[0], e[1]};
            *(LAS v2u*)(L8 + pw + 512 + p * 1024) = (v2u){e[2], e[3]};
        }
        *(LAS float*)(L8 + L_MX + (c & 7) * 16 + j3 * 4) = max_over8(rbf(m));
        lds_inc(ctl + C_MX * 4);
        v2u ea = *(LAS v2u*)(L8 + pw + j3 * 1024), eb = *(LAS v2u*)(L8 + pw + 512 + j3 * 1024);
        wait_ge(C_MX, 3);
        const v4f m3 = *(LAS v4f*)(L8 + L_MX + (c & 7) * 16);
        float mn = max3(m3[0], m3[1], m3[2]) * LOG2E;
        asm("" : "+v"(mn));
#pragma nounroll
        for (int p = j3; p < 16; p += 3) {
            const unsigned u[4] = {ea[0], ea[1], eb[0], eb[1]};
            ea = *(LAS v2u*)(L8 + pw + p * 1024 + 3072); eb = *(LAS v2u*)(L8 + pw + p * 1024 + 3584);
            float e[8];
#pragma unroll
            for (int i = 0; i < 4; ++i) {
                const v2f t = __builtin_elementwise_fma((v2f){bflo(u[i]), bfhi(u[i])}, (v2f){LOG2E, LOG2E}, (v2f){-mn, -mn});
                e[2 * i] = __builtin_amdgcn_exp2f(t[0]); e[2 * i + 1] = __builtin_amdgcn_exp2f(t[1]);
            }
            *(LAS v2u*)(L8 + pw + p * 1024) = (v2u){pack2(e[0], e[1]), pack2(e[2], e[3])};
            *(LAS v2u*)(L8 + pw + 512 + p * 1024) = (v2u){pack2(e[4], e[5]), pack2(e[6], e[7])};
            l += ((e[0] + e[1]) + (e[2] + e[3])) + ((e[4] + e[5]) + (e[6] + e[7]));
        }
        l += dpp<ROR8>(l); l = swap16_add(l, l); l = swap32_add(l, l);
        *(LAS float*)(L8 + L_MX + 128 + (c & 7) * 16 + j3 * 4) = l;
        lds_inc(ctl + C_PRDY * 4);
        wait_ge(C_PRDY, 4);
        if (wave == 5) {
            const v4f l3 = *(LAS v4f*)(L8 + L_MX + 128 + (c & 7) * 16);
            const v2u tg = *(LAS v2u*)(L8 + L_CTL + C_TAG * 4);
#pragma nounroll
            while (lds_ld(ctl + (C_NX + 1) * 4) < MLB && !spent()) nap<2>();
            const unsigned tp = (unsigned)__builtin_amdgcn_s_memrealtime();
            if (lane < 8) st16(WS_ML + r * 128 + lane * 16, (v4u){__float_as_uint(mn), __float_as_uint((l3[0] + l3[1]) + l3[2]), tg[0], (tg[1] & 0xFFFF0000u) | (tp & 0xFFFFu)});
            ST(19);
            nap<DLY_ML>();
            v4u ml = {0, 0, ~tg[0], 0};
#pragma nounroll
            for (;;) {
                CBAR();
                if (xo(ml[2], tg[0], (ml[3] ^ tg[1]) >> 16) != 0) ml = ld16(WS_ML + (g * 64 + lane) * 128 + (head & 7) * 16);
                if (wnone(xo(ml[2], tg[0], (ml[3] ^ tg[1]) >> 16) != 0) || spent()) break;
                bud -= 64;
                nap<DLY_MP>();
            }
            ST(20);
            const float ms = __uint_as_float(ml[0]);
            const float f = __builtin_amdgcn_exp2f(ms - wave_max(ms));
            *(LAS float*)(L8 + L_W + lane * 4) = f / wave_sum(f * __uint_as_float(ml[1]));
            lds_st(ctl + C_GATE * 4, tp + T1 + (int)(wave_sum((float)(short)(ml[3] - tp)) * (1.0f / 64)));
            lds_st(ctl + C_WRDY * 4, 1);
        } else {
            const int hf = wave - 6;
            unsigned va[4];
#pragma unroll
            for (int i = 0; i < 4; ++i) va[i] = trow * 256 + (((4 * hf + i) ^ (trow & 7)) << 5) + 8 * (c & 3);
            const unsigned pr = L_PF + (c & 7) * 64 + lg * 16;
            v4f acc[4];
#pragma unroll
            for (int i = 0; i < 4; ++i) acc[i] = (v4f){0, 0, 0, 0};
            unsigned po = WS_ACC + ((unsigned)(g * 8 + (c & 7)) * 64 + (r & 63)) * 256 + hf * 128 + (c >> 3) * 64 + lg * 16;
            PINV(po);
            if (hf == 0) ST(46);
#pragma nounroll
            for (int t = 0; t < 32; ++t) {
                v4u pf = *(LAS v4u*)(L8 + pr + t * 512);
                unsigned vb[4];
#pragma unroll
                for (int i = 0; i < 4; ++i) { vb[i] = va[i] + (t & 15) * 8192; PINV(vb[i]); PINV(acc[i]); }
                PINV(pf);
                if (full < 66 + 2 * t) full = wait_ge(C_FULL, 66 + 2 * t);
                CBAR();
                v4u vf[4];
#pragma unroll
                for (int i = 0; i < 4; ++i) vf[i] = trfrag(L8 + vb[i], 4096);
                lds_st(ctl + (C_NX + j3) * 4, 16 + ((t + 1) >> 1));
                SB();
#pragma unroll
                for (int i = 0; i < 4; ++i) acc[i] = mfma(vf[i], pf, acc[i]);
                SB();
            }
            const bool up = c >= 8;
            const v4f u0 = up ? acc[2] : acc[0], u1 = up ? acc[3] : acc[1];
            st16(po, (v4u){clampu(pack2(u0[0], u0[1])), clampu(pack2(u0[2], u0[3])), clampu(pack2(u1[0], u1[1])), clampu(pack2(u1[2], u1[3]))});
            ST(4 + hf);
        }
    } else {
        const int w = wave;
        const int kpos = (lane & 32) | ((lane & 12) << 1) | ((lane & 16) >> 2) | (lane & 3);
        float xres;
        {
            if (w == 0) { ST(32); STV(0, t_entry); }
            asm volatile("s_waitcnt vmcnt(8)" : "+v"(xa), "+v"(xb), "+v"(xm), "+v"(xr), "+v"(epw)::"memory");
            if (w == 0) ST(49);
            xres = bflo(xr);
            float v[16];
#pragma unroll
            for (int j = 0; j < 4; ++j) { v[2 * j] = bflo(xa[j]); v[2 * j + 1] = bfhi(xa[j]); v[8 + 2 * j] = bflo(xb[j]); v[9 + 2 * j] = bfhi(xb[j]); }
            float s = 0.f;
#pragma unroll
            for (int j = 0; j < 16; ++j) s += v[j];
            const float mean = wave_sum(s) * (1.0f / 1024);
            float q = 0.f;
#pragma unroll
            for (int j = 0; j < 16; ++j) { const float d = v[j] - mean; q += d * d; }
            const float var = wave_sum(q) * (1.0f / 1024);
            const float sd = rbf(sqrtf(rbf(rbf(var) + 1e-5f)));
            *(LAS unsigned short*)(L8 + L_XS + (w * 64 + kpos) * 2) = f2bf(rbf(bflo(xm) - rbf(mean)) / sd);
            lds_inc(ctl + C_XS * 4);
        }
        const u64 tag = ((u64)epw[0] | ((u64)epw[1] << 32)) + 0x9E3779B97F4A7C15ull;
        const v2u tg = {(unsigned)rfl((unsigned)tag), (unsigned)rfl((unsigned)(tag >> 32))};
        v4u t4 = (v4u){tg[0], tg[0] >> 16, tg[1], tg[1] >> 16} & 0xFFFu;
        unsigned fm = 0xFFFu;
        asm volatile("" : "+s"(t4), "+v"(fm));
        *(LAS v2u*)(L8 + L_CTL + C_TAG * 4) = tg;
        const int sidx = (lane & 1) ? 0xEEEE : 0x4444;
        const int spos = (lane & 13) | ((lane & 32) >> 1) | ((lane & 16) >> 3);
        const unsigned ta128 = trow * 128 + 8 * (c & 3), tx128 = (trow >> 1) & 3;
        {
            wait_ge(C_XS, 4);
            v4u xf[8];
#pragma unroll
            for (int k = 0; k < 8; ++k) xf[k] = *(LAS v4u*)(L8 + L_XS + k * 64 + lg * 16);
            if (w == 0) ST(39);
            VM0();
            lds_inc(ctl + C_WQL * 4);
            wait_ge(C_WQL, 4);
            if (w == 0) ST(40);
            v4u wf[8];
#pragma unroll
            for (int k = 0; k < 8; ++k) wf[k] = trfrag(L8 + 6 * SLOT + k * 4096 + ta128 + ((w ^ tx128) << 5), 2048);
            lds_inc(ctl + C_WQ * 4);
            v4f d0 = {0, 0, 0, 0}, d1 = {0, 0, 0, 0};
#pragma unroll
            for (int k = 0; k < 8; k += 2) { d0 = mfma(wf[k], xf[k], d0); d1 = mfma(wf[k + 1], xf[k + 1], d1); }
            d0 += d1;
            if (c < 2) st16(WS_QP + qr * 32768 + (qc * 64 + w * 16 + lg * 4 + 2 * c) * 8, (v4u){__float_as_uint(c ? d0[2] : d0[0]), __float_as_uint(c ? d0[3] : d0[1]), tg[0], tg[1]});
            if (w == 0) ST(8);
            nap<DLY_Q>();
            if (w == 0) ST(53);
            v4u q[8];
            const unsigned qo = WS_QP + g * 8192 + w * 2048 + lane * 16;
#pragma unroll
            for (int j = 0; j < 8; ++j) q[j] = ld16(qo + (j >> 1) * 32768 + (j & 1) * 1024);
#pragma nounroll
            for (;;) {
                bool nd[8]; u64 again = 0;
#pragma unroll
                for (int j = 0; j < 8; ++j) { nd[j] = xo(q[j][2], tg[0], q[j][3] ^ tg[1]) != 0; again |= __builtin_amdgcn_ballot_w64(nd[j]); }
                if (again == 0 || spent()) break;
                CBAR();
#pragma unroll
                for (int j = 0; j < 8; ++j) if (nd[j]) q[j] = ld16(qo + (j >> 1) * 32768 + (j & 1) * 1024);
            }
#pragma unroll
            for (int i = 0; i < 2; ++i) {
                const v4f s = (asf(q[i]) + asf(q[2 + i])) + (asf(q[4 + i]) + asf(q[6 + i]));
                *(LAS unsigned*)(L8 + L_QI + (2 * w + i) * 272 + lane * 4) = pack2(s[0], s[1]);
            }
            lds_inc(ctl + C_QRDY * 4);
        }
        if (w == 3) {
            const v4u sent = {SENT, SENT, SENT, SENT};
            nap<DLY_F>();
#pragma unroll
            for (int i = 0; i < 2; ++i) st16(WS_ACC + ((unsigned)(g * 8 + 4 * i + lg) * 64 + (r & 63)) * 256 + c * 16, sent);
            ST(6);
            VM0();
            lds_inc(ctl + C_PRDY * 4);
            ST(7);
        }
        if (TOUCH && w == 2) {
            nap<TOUCH>();
            if (lane < 2) {
                unsigned t0, t1, t2, t3, t4_;
                const int cf = ffn ? cb : 0;
                asm volatile("global_load_dword %0, %5, %9\n\tglobal_load_dword %1, %6, %10\n\tglobal_load_dword %2, %7, %11\n\tglobal_load_dword %3, %7, %12\n\tglobal_load_dword %4, %8, %13\n\ts_waitcnt vmcnt(0)"
                             : "=&v"(t0), "=&v"(t1), "=&v"(t2), "=&v"(t3), "=&v"(t4_)
                             : "v"(kvo + lane * (SHARE ? 512 * SLOT : 128)), "v"((head * 128 + lane * 127) * 2048 + ocb * 256), "v"((rb * 256 + lane * 255) * 7168 + cf * 128), "v"((cf * 64 + lane * 63) * 2048 + rb * 512),
                               "s"(Vc), "s"(wo), "s"(wg), "s"(wu), "s"(wd) : "memory");
            }
        }
#pragma nounroll
        while (lds_ld(ctl + (C_NX + 1) * 4) < 30 && !spent()) nap<8>();
        int gate;
        auto until = [&](int t) {
#pragma nounroll
            while ((int)((unsigned)__builtin_amdgcn_s_memrealtime() - (unsigned)gate) < t) CBAR();
        };
        {
            wait_ge(C_WRDY, 1);
            v4f w4 = *(LAS v4f*)(L8 + L_W + (16 * w + 4 * lg) * 4);
            unsigned ao = WS_ACC + (head * 64 + 16 * w + 4 * lg) * 256 + c * 16;
            PINV(w4); PINV(ao);
            gate = lds_ld(ctl + C_GATE * 4);
            if (w == 0) STV(56, (t_entry & ~0xFFFFFFFFull) | (unsigned)gate);
            wait_ge(C_NX + 1, 32);
            nap<DLY_1>();
            until(0);
            if (w == 0) ST(21);
            v4u av[4];
#pragma unroll
            for (int j = 0; j < 4; ++j) av[j] = ld16(ao + j * 256);
            lds_inc(ctl + C_WQ * 4);
            v4u wof[8];
#pragma unroll
            for (int k = 0; k < 4; ++k) {
                wait_ge(C_FULL, 130 + 2 * k);
#pragma unroll
                for (int u = 0; u < 2; ++u) wof[2 * k + u] = trfrag(L8 + k * 8192 + trow * 256 + (((4 * (w >> 1) + (c & 3)) ^ (trow & 7)) << 5) + 16 * (w & 1) + 8 * u, 4096);
            }
            if (w == 0) ST(47);
            REPOLL(4, av, ao + j * 256);
            if (w == 0) ST(9);
            v2f e[4];
#pragma unroll
            for (int i = 0; i < 4; ++i) e[i] = (v2f){bflo(av[0][i]), bfhi(av[0][i])} * w4[0];
#pragma unroll
            for (int j = 1; j < 4; ++j)
#pragma unroll
                for (int i = 0; i < 4; ++i) e[i] += (v2f){bflo(av[j][i]), bfhi(av[j][i])} * w4[j];
            *(LAS v4u*)(L8 + L_PART + (4 * w + lg) * 272 + c * 16) = (v4u){pack2(e[0][0], e[0][1]), pack2(e[1][0], e[1][1]), pack2(e[2][0], e[2][1]), pack2(e[3][0], e[3][1])};
            lds_inc(ctl + C_H1 * 4);
            v4u of[4];
#pragma nounroll
            for (;;) {
                const int n = *(volatile LAS int*)(L8 + L_CTL + C_H1 * 4);
                CBAR();
#pragma unroll
                for (int k2 = 0; k2 < 4; ++k2) of[k2] = *(LAS v4u*)(L8 + L_PART + c * 272 + k2 * 64 + lg * 16);
                SB();
                if (rfl(n) >= 4 || spent()) break;
            }
            if (w == 0) ST(48);
            v4f d0 = {0, 0, 0, 0}, d1 = {0, 0, 0, 0};
#pragma unroll
            for (int k2 = 0; k2 < 4; ++k2) { d0 = mfma(of[k2], wof[2 * k2], d0); d1 = mfma(of[k2], wof[2 * k2 + 1], d1); }
            float p = swap16_add((d0[0] + d0[1]) + (d0[2] + d0[3]), (d1[0] + d1[1]) + (d1[2] + d1[3]));
            p = swap32_add(p, p);
            const v4u rec = tagged(p, dpp<0x104>(p), dpp<0x108>(p), dpp<0x10C>(p), t4, fm);
            if ((lane & 44) == 0) st16(WS_P + head * 4096 + ocb * 512 + w * 128 + lg * 64 + c * 16, rec);
            if (w == 0) ST(10);
        }
        if (ffn) {
            v8u df[4];
            {
                v8u gf[4], uf[4];
                auto frags = [&](int i) {
                    wait_ge(C_FULL, 140 + 4 * i);
#pragma unroll
                    for (int m = 2 * (i & 1); m < 2 * (i & 1) + 2; ++m) {
                        LAS unsigned char* sp = L8 + 2 * SLOT + m * 8192 + ta128 + ((w ^ tx128) << 5);
                        if (i & 2) uf[m] = trfrag2(sp + 2 * SLOT, sp + 2 * SLOT + 4096, 2048); else gf[m] = trfrag2(sp, sp + 4096, 2048);
                    }
                };
#pragma unroll
                for (int i = 0; i < (XPOLL < 16 ? XPOLL / 4 : 4); ++i) frags(i);
                if ((XPOLL & 3) || XPOLL > 16) wait_ge(C_FULL, 136 + XPOLL);
                if (w == 0) ST(18);
                nap<DLY_2>();
                if (T2) until(T2);
                if (w == 0) ST(22);
                v4u pv[8];
                const unsigned po = WS_P + lg * 4096 + rb * 1024 + w * 256 + c * 16;
#pragma unroll
                for (int j = 0; j < 8; ++j) pv[j] = ld16(po + j * 16384);
                lds_inc(ctl + C_WQ * 4);
#pragma unroll
                for (int i = XPOLL / 4; i < 4; ++i) frags(i);
                if (w == 0) ST(23);
                TAGPOLL(8, pv, po + j * 16384, 0, 0);
                if (w == 0) ST(11);
                const v4f e = ((asf(pv[0]) + asf(pv[1])) + (asf(pv[2]) + asf(pv[3]))) + ((asf(pv[4]) + asf(pv[5])) + (asf(pv[6]) + asf(pv[7])));
                const float h = rbf(xres + rbf(rows_reduce4(e)));
                *(LAS unsigned short*)(L8 + L_HS + (lane & 2) * 136 + w * 64 + spos * 2) = f2bf(h);
                lds_inc(ctl + C_H2 * 4);
                const float piv = __uint_as_float(rfl(__float_as_uint(h))), dd = h - piv;
                const float s1 = wave_sum(dd), s2 = wave_sum(dd * dd);
                if (lane == 0) st16(WS_X + (cb * 4 + rb) * XREC + 1024 + w * 16, (v4u){__float_as_uint(s1 + 64.f * piv), __float_as_uint(s2 - s1 * s1 * (1.0f / 64)), tg[0], tg[1]});
                v4u hf[4];
#pragma nounroll
                for (;;) {
                    const int n = *(volatile LAS int*)(L8 + L_CTL + C_H2 * 4);
                    CBAR();
#pragma unroll
                    for (int m = 0; m < 4; ++m) hf[m] = *(LAS v4u*)(L8 + ((c & 2) ? L_ONE : L_HS + (c & 1) * 272) + m * 64 + lg * 16);
                    SB();
                    if (rfl(n) >= 4 || spent()) break;
                }
                v4f dg = {0, 0, 0, 0}, du = {0, 0, 0, 0};
#pragma unroll
                for (int m = 0; m < 4; ++m) { dg = smf(hf[m], gf[m], dg, sidx); du = smf(hf[m], uf[m], du, sidx); }
                if (lane < 16) st16(WS_X + (cb * 4 + rb) * XREC + w * 256 + lane * 16, tagged(dg[0] + dg[1], du[0] + du[1], dg[2] + dg[3], du[2] + du[3], t4, fm));
                if (w == 0) ST(12);
            }
            {
                nap<DLY_3>();
                if (T3) until(T3);
                v4u xv[5];
                const unsigned xa_ = WS_X + cb * 4 * XREC + lane * 16, so = WS_X + (cb * 4 + (c >> 2)) * XREC + 1024 + (c & 3) * 16;
#pragma unroll
                for (int j = 0; j < 5; ++j) xv[j] = ld16(j < 4 ? xa_ + j * XREC : so);
                wait_ge(C_FULL, 157 + w);
#pragma unroll
                for (int t = 0; t < 4; ++t) { LAS unsigned char* sp = L8 + 6 * SLOT + w * 4096 + ta128 + ((t ^ tx128) << 5); df[t] = trfrag2(sp, sp + SLOT, 2048); }
                if (w == 3) ST(55);
                TAGPOLL(4, xv, xa_ + j * XREC, 1, so);
                if (w == 0) ST(24);
                const v4f sv = asf(xv[4]);
                const float mean = row_sum(sv[0]) * (1.0f / 1024);
                const float dm = sv[0] * (1.0f / 64) - mean;
                const float m2 = row_sum(sv[1] + 64.f * dm * dm);
                const float mb = rbf(mean), isd = __builtin_amdgcn_rcpf(rbf(__builtin_amdgcn_sqrtf(rbf(rbf(m2 * (1.0f / 1024)) + 1e-5f))));
                const v4f rr = (asf(xv[0]) + asf(xv[1])) + (asf(xv[2]) + asf(xv[3]));
                const float gg = rbf((rr[0] - mb * rr[2]) * isd), uu = rbf((rr[1] - mb * rr[3]) * isd);
                const float ex = __builtin_amdgcn_exp2f((-2.0f * 0.7978845608028654f * 1.4426950408889634f) * (gg + 0.044715f * gg * gg * gg));
                *(LAS unsigned short*)(L8 + L_AS + w * 128 + (lane & 2) * 32 + spos * 2) = f2bf(rbf(gg * __builtin_amdgcn_rcpf(1.0f + ex)) * uu);
                CBAR();
                const v4u a0_ = *(LAS v4u*)(L8 + L_AS + w * 128 + (c & 1) * 64 + lg * 16);
                v4f d[4];
#pragma unroll
                for (int t = 0; t < 4; ++t) d[t] = smf(a0_, df[t], (v4f){0, 0, 0, 0}, sidx);
                if (lane < 16) st16(WS_Y + cb * 4096 + rb * 1024 + w * 256 + lane * 16, tagged(d[0][0] + d[0][1], d[1][0] + d[1][1], d[2][0] + d[2][1], d[3][0] + d[3][1], t4, fm));
                if (w == 0) ST(13);
            }
        } else if (w == 0) {
            const unsigned fo = (lane >> 3) * 4096 + fa * 128 + (lane & 7) * 16;
            nap<DLY_5>();
            if (T2) until(T2);
            v4u pv[4];
#pragma unroll
            for (int j = 0; j < 4; ++j) pv[j] = ld16(WS_P + fo + j * 32768);
            TAGPOLL(4, pv, WS_P + fo + j * 32768, 0, 0);
            if (fa == 0 && lane == 0) st8(WS_EPOCH, tg);
            float hs = rows_reduce4((asf(pv[0]) + asf(pv[1])) + (asf(pv[2]) + asf(pv[3])));
            hs += dpp<ROR8>(hs);
            const float h = rbf(xres + rbf(hs));
            ST(14);
            nap<DLY_4>();
            if (T4) until(T4);
            v4u yv[7];
#pragma unroll
            for (int j = 0; j < 7; ++j) yv[j] = ld16(WS_Y + fo + j * 32768);
            TAGPOLL(7, yv, WS_Y + fo + j * 32768, 0, 0);
            float ys = rows_reduce4(((asf(yv[0]) + asf(yv[1])) + (asf(yv[2]) + asf(yv[3]))) + ((asf(yv[4]) + asf(yv[5])) + asf(yv[6])));
            ys += dpp<ROR8>(ys);
            if (c < 8) out[fcol] = f2bf(h + rbf(ys));
            ST(15);
        }
    }
    DUMP(stm);
}

extern "C" void kernel_launch(void* const* d_in, const int* in_sizes, int n_in, void* d_out, int out_size, void* d_ws, size_t ws_size, hipStream_t stream) {
    (void)in_sizes; (void)n_in; (void)out_size; (void)ws_size;
    (void)hipFuncSetAttribute((const void*)fused, hipFuncAttributeMaxDynamicSharedMemorySize, L_END);
    fused<<<NWG, 512, L_END, stream>>>((const unsigned short*)d_in[0], (const unsigned short*)d_in[3], (unsigned char*)d_ws, (const unsigned short*)d_in[1],
                                       (const unsigned short*)d_in[2], (const unsigned short*)d_in[4], (const unsigned short*)d_in[6], (const unsigned short*)d_in[7],
                                       (const unsigned short*)d_in[8], (unsigned short*)d_out);
}
```
